```python
import torch

def _softmax(x, axis=-1):
    return torch.softmax(x, dim=axis)
def _gelu(x):
    return torch.nn.functional.gelu(x, approximate="tanh")
def _silu(x):
    return torch.nn.functional.silu(x)
def _sigmoid(x):
    return torch.sigmoid(x)
def _layernorm(x, axis=-1, eps=1e-5):
    m = x.mean(dim=axis, keepdim=True)
    v = x.var(dim=axis, keepdim=True, unbiased=False)
    return (x - m) / torch.sqrt(v + eps)
def _scatter_add(tgt, idx, src, axis=0):
    return tgt.clone().index_add_(axis, idx, src)
def _segment_sum(src, seg, n):
    out = torch.zeros((n,) + src.shape[1:], dtype=src.dtype)
    return out.index_add_(0, seg, src)
def _pad(x, pw):
    flat = tuple(v for pair in reversed(pw) for v in pair)
    return torch.nn.functional.pad(x, flat)

kernel_name = 'recurrent_scan__gqa_block_cached__mixer_token_mlp__decode'

def setup_inputs():

    _w = (lambda a: (lambda t: t.to(torch.bfloat16) if t.is_floating_point() else t)(torch.as_tensor(a)))
    def _A_setup():
        B, Sq, D = 4, 1, 576
        Hq, Hkv, Sk, Dh = 32, 16, 8192, 128
        assert Hq % Hkv == 0, (Hq, Hkv)
        return {
            "x": _w((lambda *s: torch.randn(*s, dtype=torch.bfloat16) * 0.05)(B, Sq, D)),
            "k_cache": _w((lambda *s: torch.randn(*s, dtype=torch.bfloat16) * 1.6)(B, Hkv, Sk, Dh)),
            "v_cache": _w((lambda *s: torch.randn(*s, dtype=torch.bfloat16) * 1.6)(B, Hkv, Sk, Dh)),
            "wq": _w((lambda *s: torch.randn(*s, dtype=torch.bfloat16) * 1.6)(D, Hq * Dh) * (D ** -0.5)),
            "wo": _w((lambda *s: torch.randn(*s, dtype=torch.bfloat16) * 1.6)(Hq * Dh, D) * ((Hq * Dh) ** -0.5)),
        }
    _A_in = _A_setup()

    def _B_setup():
        B, S, D, F = 4, 1, 576, 4608
        return {
            "x": _w((lambda *s: torch.randn(*s, dtype=torch.bfloat16) * 0.05)(B, S, D)),
            "w1": _w((lambda *s: torch.randn(*s, dtype=torch.bfloat16) * 0.2)(S, F) * (S ** -0.5)),
            "b1": _w((lambda *s: torch.randn(*s, dtype=torch.bfloat16) * 0.2)(F) * 0.02),
            "w2": _w((lambda *s: torch.randn(*s, dtype=torch.bfloat16) * 0.2)(F, S) * (F ** -0.5)),
            "b2": _w((lambda *s: torch.randn(*s, dtype=torch.bfloat16) * 0.2)(S) * 0.02),
        }
    _B_in = _B_setup()

    return {
        "A_x": _A_in["x"],
        "A_k_cache": _A_in["k_cache"],
        "A_v_cache": _A_in["v_cache"],
        "A_wq": _A_in["wq"],
        "A_wo": _A_in["wo"],
        "B_x": _B_in["x"],
        "B_w1": _B_in["w1"],
        "B_b1": _B_in["b1"],
        "B_w2": _B_in["w2"],
        "B_b2": _B_in["b2"],
    }

def reference(A_x, A_k_cache, A_v_cache, A_wq, A_wo, B_x, B_w1, B_b1, B_w2, B_b2):
    def _A_ref(x, k_cache, v_cache, wq, wo):
        B, Sq, D = x.shape
        Hq, Dh = 32, 128
        Hkv = k_cache.shape[1]
        rep = Hq // Hkv
        q = (lambda x, *d: x.permute(*d))((x @ wq).reshape(B, Sq, Hq, Dh), 0, 2, 1, 3)
        k = (lambda x, n, axis: torch.repeat_interleave(x, n, dim=axis))(k_cache, rep, axis=1)
        v = (lambda x, n, axis: torch.repeat_interleave(x, n, dim=axis))(v_cache, rep, axis=1)
        scores = torch.einsum("bhqd,bhkd->bhqk", q, k) * (Dh ** -0.5)
        p = _softmax(scores, axis=-1)
        o = torch.einsum("bhqk,bhkd->bhqd", p, v)
        return (lambda x, *d: x.permute(*d))(o, 0, 2, 1, 3).reshape(B, Sq, Hq * Dh) @ wo

    def _B_ref(x, w1, b1, w2, b2):
        B, S, D = x.shape
        h = _layernorm(x, axis=-1)
        u = torch.einsum("bsd,sf->bdf", h, w1) + b1
        u = _gelu(u)
        y = torch.einsum("bdf,fs->bds", u, w2) + b2
        return (lambda x, *d: x.permute(*d))(y, 0, 2, 1)

    x = A_x
    xn = _layernorm(x, axis=-1)
    s = _A_ref(xn, A_k_cache, A_v_cache, A_wq, A_wo)
    g = _silu(_B_ref(xn, B_w1, B_b1, B_w2, B_b2))
    return x + s * g
```

```cpp
#include <hip/hip_runtime.h>
#include <hip/hip_bf16.h>
#include <stdint.h>

#define GAS __attribute__((address_space(1)))
typedef unsigned u32x4 __attribute__((ext_vector_type(4)));
typedef unsigned u32x2 __attribute__((ext_vector_type(2)));
typedef float f32x4 __attribute__((ext_vector_type(4)));
typedef short bf16x8 __attribute__((ext_vector_type(8)));
typedef __bf16 bf16x2 __attribute__((ext_vector_type(2)));

constexpr int NB = 4, DM = 576, HQ = 32, HKV = 16, SK = 8192, DH = 128, FF = 4608;
constexpr int NWG = 256;
#define NWAVES 8
#define DEPTH 3
constexpr int NW = NWAVES, NTHR = NW * 64;
constexpr int NSPLIT = 4, KEYS_WG = SK / NSPLIT;
constexpr int NT = KEYS_WG / (16 * NW);
constexpr float C2 = 0.088388347648318440f * 1.4426950408889634f;

constexpr size_t WS_Q = 0;
constexpr size_t WS_G = 65536;
constexpr size_t WS_P = 131072;
constexpr size_t WS_O = WS_P + (size_t)NB * HQ * NSPLIT * 132 * 4;

__device__ __forceinline__ float bf2f(unsigned short v) { return __builtin_bit_cast(float, (unsigned)v << 16); }
__device__ __forceinline__ float bflo(unsigned v) { return __builtin_bit_cast(float, v << 16); }
__device__ __forceinline__ float bfhi(unsigned v) { return __builtin_bit_cast(float, v & 0xffff0000u); }
__device__ __forceinline__ unsigned cvtpk(float lo, float hi) {
  bf16x2 r = {(__bf16)lo, (__bf16)hi};
  return __builtin_bit_cast(unsigned, r);
}
__device__ __forceinline__ float dot2(unsigned k, unsigned q, float acc) {
  return __builtin_amdgcn_fdot2_f32_bf16(__builtin_bit_cast(bf16x2, k), __builtin_bit_cast(bf16x2, q), acc, false);
}
template <int CTRL> __device__ __forceinline__ float dpp(float x) {
  return __builtin_bit_cast(float, __builtin_amdgcn_mov_dpp(__builtin_bit_cast(int, x), CTRL, 0xf, 0xf, true));
}
constexpr int XOR1 = 0xB1, XOR2 = 0x4E, XOR7 = 0x141, ROR8 = 0x128;
__device__ __forceinline__ float xrow_max(float x) {
  auto s = __builtin_amdgcn_permlane16_swap(__float_as_uint(x), __float_as_uint(x), false, false);
  x = fmaxf(__uint_as_float(s[0]), __uint_as_float(s[1]));
  auto t = __builtin_amdgcn_permlane32_swap(__float_as_uint(x), __float_as_uint(x), false, false);
  return fmaxf(__uint_as_float(t[0]), __uint_as_float(t[1]));
}
__device__ __forceinline__ float xrow_sum(float x) {
  auto s = __builtin_amdgcn_permlane16_swap(__float_as_uint(x), __float_as_uint(x), false, false);
  x = __uint_as_float(s[0]) + __uint_as_float(s[1]);
  auto t = __builtin_amdgcn_permlane32_swap(__float_as_uint(x), __float_as_uint(x), false, false);
  return __uint_as_float(t[0]) + __uint_as_float(t[1]);
}
__device__ __forceinline__ float wave_sum(float x) {
  x += dpp<XOR1>(x); x += dpp<XOR2>(x); x += dpp<XOR7>(x); x += dpp<ROR8>(x);
  return xrow_sum(x);
}
__device__ __forceinline__ f32x4 mfma16(u32x4 a, u32x4 b, f32x4 c) {
  return __builtin_amdgcn_mfma_f32_16x16x32_bf16(__builtin_bit_cast(bf16x8, a), __builtin_bit_cast(bf16x8, b), c, 0, 0, 0);
}
__device__ __forceinline__ u32x4 ldnt(const GAS u32x4* p) { return __builtin_nontemporal_load(p); }

struct Ids { int kvh, i; };
__device__ __forceinline__ Ids wg_ids(int j) { Ids r; r.kvh = 2 * (j & 7) + ((j >> 3) & 1); r.i = j >> 4; return r; }

__global__ void __launch_bounds__(512) k_prep(const unsigned short* __restrict__ x, const unsigned short* __restrict__ wq,
                                              const unsigned short* __restrict__ w1, const unsigned short* __restrict__ b1,
                                              const unsigned short* __restrict__ w2, const unsigned short* __restrict__ b2,
                                              unsigned char* __restrict__ ws) {
  __shared__ __attribute__((aligned(16))) unsigned short xn_s[NB][DM];
  __shared__ float st2[NB][2];
  __shared__ float red[8][64];
  __shared__ float red2[8][12];
  const int tid = threadIdx.x, lane = tid & 63, w = __builtin_amdgcn_readfirstlane(tid >> 6);
  const Ids id = wg_ids(blockIdx.x);
  const int ncol = 256 * id.kvh + 16 * id.i + (lane & 15);
  unsigned short bw[3][8];
#pragma unroll
  for (int s = 0; s < 3; ++s) {
    const int ks = w + 8 * s;
    if (ks < 18) {
#pragma unroll
      for (int j = 0; j < 8; ++j) bw[s][j] = wq[(size_t)(32 * ks + 8 * (lane >> 4) + j) * 4096 + ncol];
    } else {
#pragma unroll
      for (int j = 0; j < 8; ++j) bw[s][j] = 0;
    }
  }
  if (w < 4) {
    const bool act = lane < 36;
    u32x4 v0 = {0, 0, 0, 0}, v1 = {0, 0, 0, 0};
    if (act) {
      v0 = *(const GAS u32x4*)(x + w * DM + 8 * lane);
      v1 = *(const GAS u32x4*)(x + w * DM + 8 * (lane + 36));
    }
    float f[16];
#pragma unroll
    for (int k = 0; k < 4; ++k) { f[2 * k] = bflo(v0[k]); f[2 * k + 1] = bfhi(v0[k]); f[8 + 2 * k] = bflo(v1[k]); f[8 + 2 * k + 1] = bfhi(v1[k]); }
    float s = 0.f;
#pragma unroll
    for (int k = 0; k < 16; ++k) s += f[k];
    const float mean = wave_sum(s) * (1.f / DM);
    float q = 0.f;
#pragma unroll
    for (int k = 0; k < 16; ++k) { f[k] -= mean; q += act ? f[k] * f[k] : 0.f; }
    const float rstd = 1.0f / sqrtf(wave_sum(q) * (1.f / DM) + 1e-5f);
    u32x4 o0, o1;
#pragma unroll
    for (int k = 0; k < 4; ++k) { o0[k] = cvtpk(f[2 * k] * rstd, f[2 * k + 1] * rstd); o1[k] = cvtpk(f[8 + 2 * k] * rstd, f[8 + 2 * k + 1] * rstd); }
    if (act) { *(u32x4*)&xn_s[w][8 * lane] = o0; *(u32x4*)&xn_s[w][8 * (lane + 36)] = o1; }
#pragma unroll
    for (int k = 0; k < 4; ++k) { f[2 * k] = bflo(o0[k]); f[2 * k + 1] = bfhi(o0[k]); f[8 + 2 * k] = bflo(o1[k]); f[8 + 2 * k + 1] = bfhi(o1[k]); }
    s = 0.f;
#pragma unroll
    for (int k = 0; k < 16; ++k) s += act ? f[k] : 0.f;
    const float mean2 = wave_sum(s) * (1.f / DM);
    q = 0.f;
#pragma unroll
    for (int k = 0; k < 16; ++k) { const float d = f[k] - mean2; q += act ? d * d : 0.f; }
    const float rstd2 = 1.0f / sqrtf(wave_sum(q) * (1.f / DM) + 1e-5f);
    if (lane == 0) { st2[w][0] = mean2; st2[w][1] = rstd2; }
  }
  __syncthreads();
  f32x4 acc = {0.f, 0.f, 0.f, 0.f};
#pragma unroll
  for (int s = 0; s < 3; ++s) {
    const int ks = w + 8 * s;
    if (ks < 18) {
      u32x4 a = {0, 0, 0, 0};
      if ((lane & 15) < 4) a = *(const u32x4*)&xn_s[lane & 15][32 * ks + 8 * (lane >> 4)];
      u32x4 bb;
#pragma unroll
      for (int k = 0; k < 4; ++k) bb[k] = (unsigned)bw[s][2 * k] | ((unsigned)bw[s][2 * k + 1] << 16);
      acc = mfma16(a, bb, acc);
    }
  }
  if (lane < 16) {
#pragma unroll
    for (int b = 0; b < 4; ++b) red[w][b * 16 + lane] = acc[b];
  }
  const int h = 2 * id.kvh + (id.i >> 3), cg = id.i & 7;
  const int bo = h >> 3, d0 = 72 * cg + 9 * (h & 7);
  float hv[9], ya[9];
#pragma unroll
  for (int u = 0; u < 9; ++u) { hv[u] = bflo(cvtpk((bf2f(xn_s[bo][d0 + u]) - st2[bo][0]) * st2[bo][1], 0.f)); ya[u] = 0.f; }
  constexpr float G0 = -2.0f * 0.7978845608028654f * 1.4426950408889634f, G1 = G0 * 0.044715f;
#pragma unroll
  for (int k = 0; k < 9; ++k) {
    const int f = tid + 512 * k;
    const float a1 = bf2f(w1[f]), c1 = bf2f(b1[f]), a2 = bf2f(w2[f]);
#pragma unroll
    for (int u = 0; u < 9; ++u) {
      const float uu = fmaf(hv[u], a1, c1);
      const float t = fmaf(uu * uu, G1, G0);
      const float e = __builtin_amdgcn_exp2f(uu * t);
      const float gl = uu * __builtin_amdgcn_rcpf(1.0f + e);
      ya[u] = fmaf(gl, a2, ya[u]);
    }
  }
#pragma unroll
  for (int u = 0; u < 9; ++u) { const float s = wave_sum(ya[u]); if (lane == 0) red2[w][u] = s; }
  __syncthreads();
  if (tid < 64) {
    float s = 0.f;
#pragma unroll
    for (int ww = 0; ww < 8; ++ww) s += red[ww][tid];
    const int b = tid >> 4;
    ((unsigned short*)(ws + WS_Q))[b * 4096 + 256 * id.kvh + 16 * id.i + (tid & 15)] = (unsigned short)(cvtpk(s, 0.f) & 0xffffu);
  } else if (tid < 64 + 9) {
    const int u = tid - 64;
    float s = bf2f(b2[0]);
#pragma unroll
    for (int ww = 0; ww < 8; ++ww) s += red2[ww][u];
    const float g = s / (1.0f + __expf(-s));
    ((float*)(ws + WS_G))[bo * DM + d0 + u] = g;
  }
}

__global__ void __launch_bounds__(NTHR) k_attn(const unsigned short* __restrict__ kc, const unsigned short* __restrict__ vc,
                                               unsigned char* __restrict__ ws) {
  __shared__ __attribute__((aligned(16))) float mrg[NW][260];
  const int tid = threadIdx.x, lane = tid & 63, w = __builtin_amdgcn_readfirstlane(tid >> 6);
  const Ids id = wg_ids(blockIdx.x);
  const int b = id.i >> 2, split = id.i & 3, kvh = id.kvh;
  const int c = lane & 15, hl = c >> 3;
  const size_t base = ((size_t)(b * HKV + kvh) * SK + (size_t)split * KEYS_WG + (size_t)w * 16) * DH;
  const GAS u32x4* kp = (const GAS u32x4*)(kc + base) + lane;
  const GAS u32x4* vp = (const GAS u32x4*)(vc + base) + lane;
  constexpr int TSTR = NW * 16 * DH / 8;
  u32x4 Kr[DEPTH][4], Vr[DEPTH][4];
  const unsigned short* q = (const unsigned short*)(ws + WS_Q) + b * 4096 + kvh * 256;
  const u32x4 qa = *(const GAS u32x4*)(q + hl * 128 + 8 * c);
  const u32x4 qb = *(const GAS u32x4*)(q + (1 - hl) * 128 + 8 * c);
#pragma unroll
  for (int d = 0; d < DEPTH; ++d) {
#pragma unroll
    for (int t = 0; t < 4; ++t) Kr[d][t] = ldnt(kp + d * TSTR + t * 64);
#pragma unroll
    for (int t = 0; t < 4; ++t) Vr[d][t] = ldnt(vp + d * TSTR + t * 64);
  }
  __builtin_amdgcn_sched_barrier(0);
  unsigned msk[4];
#pragma unroll
  for (int k = 0; k < 4; ++k) msk[k] = ((c & 7) >> 1) == k ? ((c & 1) ? 0xffff0000u : 0x0000ffffu) : 0u;
  f32x4 acc = {0.f, 0.f, 0.f, 0.f};
  float m = -INFINITY, l = 0.f;
#pragma unroll
  for (int it = 0; it < NT; ++it) {
    const int sl = it % DEPTH;
    float s[4];
#pragma unroll
    for (int t = 0; t < 4; ++t) {
      float xa = 0.f, xb = 0.f;
#pragma unroll
      for (int k = 0; k < 4; ++k) { xa = dot2(Kr[sl][t][k], qa[k], xa); xb = dot2(Kr[sl][t][k], qb[k], xb); }
      float y = xa + dpp<ROR8>(xb);
      y += dpp<XOR7>(y); y += dpp<XOR1>(y); y += dpp<XOR2>(y);
      s[t] = y;
    }
    if (it + DEPTH < NT) {
#pragma unroll
      for (int t = 0; t < 4; ++t) Kr[sl][t] = ldnt(kp + (it + DEPTH) * TSTR + t * 64);
    }
    __builtin_amdgcn_sched_barrier(0);
    float mt = fmaxf(fmaxf(s[0], s[1]), fmaxf(s[2], s[3]));
    mt = xrow_max(mt);
    const float mn = fmaxf(m, mt);
    const float alpha = __builtin_amdgcn_exp2f((m - mn) * C2);
    m = mn;
    const float mc = -mn * C2;
    acc *= alpha;
    l *= alpha;
#pragma unroll
    for (int t = 0; t < 4; ++t) {
      const float p = __builtin_amdgcn_exp2f(fmaf(s[t], C2, mc));
      l += p;
      const unsigned pp = cvtpk(p, p);
      const u32x4 bb = {pp & msk[0], pp & msk[1], pp & msk[2], pp & msk[3]};
      acc = mfma16(Vr[sl][t], bb, acc);
    }
    if (it + DEPTH < NT) {
#pragma unroll
      for (int t = 0; t < 4; ++t) Vr[sl][t] = ldnt(vp + (it + DEPTH) * TSTR + t * 64);
    }
    __builtin_amdgcn_sched_barrier(0);
  }
  l = xrow_sum(l);
#pragma unroll
  for (int i = 0; i < 4; ++i) mrg[w][4 + hl * 128 + 8 * (4 * (lane >> 4) + i) + (lane & 7)] = acc[i];
  if ((lane & 55) == 0) { mrg[w][hl * 2] = m; mrg[w][hl * 2 + 1] = l; }
  __syncthreads();
  if (tid < 256) {
    const int hh = tid >> 7, d = tid & 127;
    float M = -INFINITY;
#pragma unroll
    for (int ww = 0; ww < NW; ++ww) M = fmaxf(M, mrg[ww][hh * 2]);
    float L = 0.f, o = 0.f;
#pragma unroll
    for (int ww = 0; ww < NW; ++ww) {
      const float wt = __builtin_amdgcn_exp2f((mrg[ww][hh * 2] - M) * C2);
      L = fmaf(wt, mrg[ww][hh * 2 + 1], L);
      o = fmaf(wt, mrg[ww][4 + tid], o);
    }
    float* pr = (float*)(ws + WS_P) + ((size_t)(b * HQ + 2 * kvh + hh) * NSPLIT + split) * 132;
    pr[4 + d] = o;
    if (d == 0) { pr[0] = M; pr[1] = L; }
  }
}

__global__ void __launch_bounds__(512) k_wo(const unsigned short* __restrict__ wo, unsigned char* __restrict__ ws) {
  __shared__ __attribute__((aligned(16))) unsigned short o_s[NB][DH];
  const int tid = threadIdx.x, lane = tid & 63, w = __builtin_amdgcn_readfirstlane(tid >> 6);
  const Ids id = wg_ids(blockIdx.x);
  const int h = 2 * id.kvh + (id.i >> 3), cg = id.i & 7;
  u32x4 bb[4];
  const int n = 16 * w + (lane & 15);
  if (w < 5) {
#pragma unroll
    for (int ks = 0; ks < 4; ++ks) {
      unsigned short t[8];
#pragma unroll
      for (int j = 0; j < 8; ++j) t[j] = n < 72 ? wo[(size_t)(128 * h + 32 * ks + 8 * (lane >> 4) + j) * DM + 72 * cg + n] : (unsigned short)0;
#pragma unroll
      for (int k = 0; k < 4; ++k) bb[ks][k] = (unsigned)t[2 * k] | ((unsigned)t[2 * k + 1] << 16);
    }
  }
  {
    const int b = tid >> 7, d = tid & 127;
    const float* pr = (const float*)(ws + WS_P) + ((size_t)(b * HQ + h) * NSPLIT) * 132;
    float M = -INFINITY;
#pragma unroll
    for (int s = 0; s < NSPLIT; ++s) M = fmaxf(M, pr[s * 132]);
    float L = 0.f, o = 0.f;
#pragma unroll
    for (int s = 0; s < NSPLIT; ++s) {
      const float wt = __builtin_amdgcn_exp2f((pr[s * 132] - M) * C2);
      L = fmaf(wt, pr[s * 132 + 1], L);
      o = fmaf(wt, pr[s * 132 + 4 + d], o);
    }
    o_s[b][d] = (unsigned short)(cvtpk(o / L, 0.f) & 0xffffu);
  }
  __syncthreads();
  if (w < 5) {
    f32x4 acc = {0.f, 0.f, 0.f, 0.f};
#pragma unroll
    for (int ks = 0; ks < 4; ++ks) {
      u32x4 a = {0, 0, 0, 0};
      if ((lane & 15) < 4) a = *(const u32x4*)&o_s[lane & 15][32 * ks + 8 * (lane >> 4)];
      acc = mfma16(a, bb[ks], acc);
    }
    if (lane < 16 && n < 72) {
      float* op = (float*)(ws + WS_O) + (size_t)h * NB * DM + 72 * cg + n;
#pragma unroll
      for (int b = 0; b < 4; ++b) op[b * DM] = acc[b];
    }
  }
}

__global__ void __launch_bounds__(256) k_final(const unsigned short* __restrict__ x, const unsigned char* __restrict__ ws, unsigned short* __restrict__ out) {
  const int e = blockIdx.x * 256 + threadIdx.x;
  if (e >= NB * DM) return;
  const float* op = (const float*)(ws + WS_O) + e;
  float s = 0.f;
#pragma unroll
  for (int h = 0; h < HQ; ++h) s += op[(size_t)h * NB * DM];
  const float g = ((const float*)(ws + WS_G))[e];
  out[e] = (unsigned short)(cvtpk(bf2f(x[e]) + s * g, 0.f) & 0xffffu);
}

extern "C" void kernel_launch(void* const* d_in, const int* in_sizes, int n_in, void* d_out, int out_size, void* d_ws, size_t ws_size,
                              hipStream_t stream) {
  const unsigned short* x = (const unsigned short*)d_in[0];
  const unsigned short* kc = (const unsigned short*)d_in[1];
  const unsigned short* vc = (const unsigned short*)d_in[2];
  const unsigned short* wq = (const unsigned short*)d_in[3];
  const unsigned short* wo = (const unsigned short*)d_in[4];
  const unsigned short* w1 = (const unsigned short*)d_in[6];
  const unsigned short* b1 = (const unsigned short*)d_in[7];
  const unsigned short* w2 = (const unsigned short*)d_in[8];
  const unsigned short* b2 = (const unsigned short*)d_in[9];
  unsigned char* ws = (unsigned char*)d_ws;
  k_prep<<<NWG, 512, 0, stream>>>(x, wq, w1, b1, w2, b2, ws);
  k_attn<<<NWG, NTHR, 0, stream>>>(kc, vc, ws);
  k_wo<<<NWG, 512, 0, stream>>>(wo, ws);
  k_final<<<9, 256, 0, stream>>>(x, ws, (unsigned short*)d_out);
}
```

```cpp
#include <hip/hip_runtime.h>
#include <hip/hip_bf16.h>
#include <stdint.h>

#define GAS __attribute__((address_space(1)))
typedef unsigned u32x4 __attribute__((ext_vector_type(4)));
typedef unsigned u32x2 __attribute__((ext_vector_type(2)));
typedef float f32x4 __attribute__((ext_vector_type(4)));
typedef short bf16x8 __attribute__((ext_vector_type(8)));
typedef __bf16 bf16x2 __attribute__((ext_vector_type(2)));
typedef unsigned long long u64;

constexpr int NB = 4, DM = 576, HQ = 32, HKV = 16, SK = 8192, DH = 128, FF = 4608;
constexpr int NWG = 256, NW = 8, NTHR = 512;
#define DEPTH 4
constexpr int NSPLIT = 4, KEYS_WG = SK / NSPLIT;
constexpr int NT = KEYS_WG / (16 * NW);
constexpr float C2 = 0.088388347648318440f * 1.4426950408889634f;
constexpr unsigned SPIN_CAP = 1u << 16;

constexpr int WS_E = 0;
constexpr int WS_Q0 = 4096;
constexpr int WS_P1A = WS_Q0 + NB * HKV * 256 * 8;
constexpr int WS_P1M = WS_P1A + HQ * NB * NSPLIT * 128 * 8;
constexpr int WS_P2 = WS_P1M + HQ * NB * NSPLIT * 2 * 8;
constexpr int WS_END = WS_P2 + 8 * HQ * NB * 72 * 8;
constexpr int WS_STAMP = 4 << 20;
constexpr int AUX_SC1 = 16;

__device__ __forceinline__ float bf2f(unsigned short v) { return __builtin_bit_cast(float, (unsigned)v << 16); }
__device__ __forceinline__ float bflo(unsigned v) { return __builtin_bit_cast(float, v << 16); }
__device__ __forceinline__ float bfhi(unsigned v) { return __builtin_bit_cast(float, v & 0xffff0000u); }
__device__ __forceinline__ unsigned cvtpk(float lo, float hi) {
  bf16x2 r = {(__bf16)lo, (__bf16)hi};
  return __builtin_bit_cast(unsigned, r);
}
__device__ __forceinline__ float dot2(unsigned k, unsigned q, float acc) {
  return __builtin_amdgcn_fdot2_f32_bf16(__builtin_bit_cast(bf16x2, k), __builtin_bit_cast(bf16x2, q), acc, false);
}
template <int CTRL> __device__ __forceinline__ float dpp(float x) {
  return __builtin_bit_cast(float, __builtin_amdgcn_mov_dpp(__builtin_bit_cast(int, x), CTRL, 0xf, 0xf, true));
}
constexpr int XOR1 = 0xB1, XOR2 = 0x4E, XOR7 = 0x141, ROR8 = 0x128;
__device__ __forceinline__ float x16_sum(float x) {
  auto s = __builtin_amdgcn_permlane16_swap(__float_as_uint(x), __float_as_uint(x), false, false);
  return __uint_as_float(s[0]) + __uint_as_float(s[1]);
}
__device__ __forceinline__ float xrow_max(float x) {
  auto s = __builtin_amdgcn_permlane16_swap(__float_as_uint(x), __float_as_uint(x), false, false);
  x = fmaxf(__uint_as_float(s[0]), __uint_as_float(s[1]));
  auto t = __builtin_amdgcn_permlane32_swap(__float_as_uint(x), __float_as_uint(x), false, false);
  return fmaxf(__uint_as_float(t[0]), __uint_as_float(t[1]));
}
__device__ __forceinline__ float xrow_sum(float x) {
  x = x16_sum(x);
  auto t = __builtin_amdgcn_permlane32_swap(__float_as_uint(x), __float_as_uint(x), false, false);
  return __uint_as_float(t[0]) + __uint_as_float(t[1]);
}
__device__ __forceinline__ float row_sum(float x) {
  x += dpp<XOR1>(x); x += dpp<XOR2>(x); x += dpp<XOR7>(x); x += dpp<ROR8>(x);
  return x;
}
__device__ __forceinline__ float wave_sum(float x) { return xrow_sum(row_sum(x)); }
__device__ __forceinline__ f32x4 mfma16(u32x4 a, u32x4 b, f32x4 c) {
  return __builtin_amdgcn_mfma_f32_16x16x32_bf16(__builtin_bit_cast(bf16x8, a), __builtin_bit_cast(bf16x8, b), c, 0, 0, 0);
}
__device__ __forceinline__ u32x4 ldnt(const GAS u32x4* p) { return __builtin_nontemporal_load(p); }
#define SB() __builtin_amdgcn_sched_barrier(0)
#define ORDER() __builtin_amdgcn_fence(__ATOMIC_ACQUIRE, "wavefront")

#define STAMP(k) do {} while (0)

struct Lds {
  unsigned short xn[NB][DM];
  unsigned short q[256];
  unsigned short o[NB][DH];
  float st2[NB][2];
  float red[NW][64];
  float red2[NW][12];
  float mrg[NW][260];
};

__global__ void __launch_bounds__(NTHR, 2)
k_fused(const unsigned short* __restrict__ x, const unsigned short* __restrict__ kc, const unsigned short* __restrict__ vc,
        const unsigned short* __restrict__ wq, const unsigned short* __restrict__ wo, const unsigned short* __restrict__ w1,
        const unsigned short* __restrict__ b1, const unsigned short* __restrict__ w2, const unsigned short* __restrict__ b2,
        unsigned char* ws, unsigned short* __restrict__ out) {
  __shared__ __attribute__((aligned(16))) Lds L;
  const int tid = threadIdx.x, lane = tid & 63, w = __builtin_amdgcn_readfirstlane(tid >> 6);
  const int jb = blockIdx.x;
  const int kvh = 2 * (jb & 7) + ((jb >> 3) & 1), gi = jb >> 4;
  const int b = gi >> 2, split = gi & 3;
  const int h = 2 * kvh + (gi >> 3), cg = gi & 7;
  const int bo = h >> 3, d0 = 72 * cg + 9 * (h & 7);
  const int c = lane & 15, hl = c >> 3;
  const __amdgpu_buffer_rsrc_t rs = __builtin_amdgcn_make_buffer_rsrc((void*)ws, 0, WS_END, 0x00020000);
  STAMP(0);

  {
    const u32x4 z = {0u, 0u, 0u, 0u};
    if (w == 4 || w == 5) {
      __builtin_amdgcn_raw_buffer_store_b128(z, rs, WS_P1A + (((2 * kvh + (w - 4)) * NB + b) * NSPLIT + split) * 1024 + lane * 16, 0, AUX_SC1);
      if (lane == 0) __builtin_amdgcn_raw_buffer_store_b128(z, rs, WS_P1M + (((2 * kvh + (w - 4)) * NB + b) * NSPLIT + split) * 16, 0, AUX_SC1);
    } else if (w >= 6) {
      const int o = WS_P2 + (cg * HQ + h) * 2304;
      __builtin_amdgcn_raw_buffer_store_b128(z, rs, o + (tid - 384) * 16, 0, AUX_SC1);
      if (tid < 384 + 16) __builtin_amdgcn_raw_buffer_store_b128(z, rs, o + (tid - 384 + 128) * 16, 0, AUX_SC1);
    }
  }
  const u32x2 ev = __builtin_amdgcn_raw_buffer_load_b64(rs, WS_E, 0, AUX_SC1);
  u32x4 xv0 = {0, 0, 0, 0}, xv1 = {0, 0, 0, 0};
  const bool xact = (w < 4) && (lane < 36);
  if (xact) {
    xv0 = *(const GAS u32x4*)(x + w * DM + 8 * lane);
    xv1 = *(const GAS u32x4*)(x + w * DM + 8 * (lane + 36));
  }
  unsigned short bw[3][8];
  {
    const GAS unsigned short* wp = (const GAS unsigned short*)wq + (size_t)(72 * w) * 4096 + 256 * kvh + 16 * gi + c;
#pragma unroll
    for (int s = 0; s < 2; ++s)
#pragma unroll
      for (int j = 0; j < 8; ++j) bw[s][j] = wp[(size_t)(32 * s + 8 * (lane >> 4) + j) * 4096];
#pragma unroll
    for (int j = 0; j < 8; ++j) bw[2][j] = wp[(size_t)(64 + j) * 4096];
  }
  const u32x4 m1v = *(const GAS u32x4*)(w1 + 8 * tid), mbv = *(const GAS u32x4*)(b1 + 8 * tid), m2v = *(const GAS u32x4*)(w2 + 8 * tid);
  const unsigned short m1s = ((const GAS unsigned short*)w1)[4096 + tid], mbs = ((const GAS unsigned short*)b1)[4096 + tid],
                       m2s = ((const GAS unsigned short*)w2)[4096 + tid];
  SB();
  const size_t base = ((size_t)(b * HKV + kvh) * SK + (size_t)split * KEYS_WG + (size_t)w * 16) * DH;
  const GAS u32x4* kp = (const GAS u32x4*)(kc + base) + lane;
  const GAS u32x4* vp = (const GAS u32x4*)(vc + base) + lane;
  constexpr int TSTR = NW * 16 * DH / 8;
  u32x4 Kr[DEPTH][4], Vr[DEPTH][4];
#define ISSUE_K(sl, tile) do { _Pragma("unroll") for (int t_ = 0; t_ < 4; ++t_) Kr[sl][t_] = ldnt(kp + (tile) * TSTR + t_ * 64); } while (0)
#define ISSUE_V(sl, tile) do { _Pragma("unroll") for (int t_ = 0; t_ < 4; ++t_) Vr[sl][t_] = ldnt(vp + (tile) * TSTR + t_ * 64); } while (0)
  ISSUE_K(0, 0); ISSUE_V(0, 0);
  SB();

  if (w < 4) {
    float f[16];
#pragma unroll
    for (int k = 0; k < 4; ++k) { f[2 * k] = bflo(xv0[k]); f[2 * k + 1] = bfhi(xv0[k]); f[8 + 2 * k] = bflo(xv1[k]); f[8 + 2 * k + 1] = bfhi(xv1[k]); }
    float s = 0.f;
#pragma unroll
    for (int k = 0; k < 16; ++k) s += f[k];
    const float mean = wave_sum(s) * (1.f / DM);
    float q = 0.f;
#pragma unroll
    for (int k = 0; k < 16; ++k) { f[k] -= mean; q += xact ? f[k] * f[k] : 0.f; }
    const float rstd = 1.0f / sqrtf(wave_sum(q) * (1.f / DM) + 1e-5f);
    u32x4 o0, o1;
#pragma unroll
    for (int k = 0; k < 4; ++k) { o0[k] = cvtpk(f[2 * k] * rstd, f[2 * k + 1] * rstd); o1[k] = cvtpk(f[8 + 2 * k] * rstd, f[8 + 2 * k + 1] * rstd); }
    if (xact) { *(u32x4*)&L.xn[w][8 * lane] = o0; *(u32x4*)&L.xn[w][8 * (lane + 36)] = o1; }
#pragma unroll
    for (int k = 0; k < 4; ++k) { f[2 * k] = bflo(o0[k]); f[2 * k + 1] = bfhi(o0[k]); f[8 + 2 * k] = bflo(o1[k]); f[8 + 2 * k + 1] = bfhi(o1[k]); }
    s = 0.f;
#pragma unroll
    for (int k = 0; k < 16; ++k) s += xact ? f[k] : 0.f;
    const float mean2 = wave_sum(s) * (1.f / DM);
    q = 0.f;
#pragma unroll
    for (int k = 0; k < 16; ++k) { const float d = f[k] - mean2; q += xact ? d * d : 0.f; }
    const float rstd2 = 1.0f / sqrtf(wave_sum(q) * (1.f / DM) + 1e-5f);
    if (lane == 0) { L.st2[w][0] = mean2; L.st2[w][1] = rstd2; }
  }
  STAMP(1);
  __syncthreads();
  {
    f32x4 acc = {0.f, 0.f, 0.f, 0.f};
#pragma unroll
    for (int s = 0; s < 3; ++s) {
      const bool av = (c < 4) && (s < 2 || lane < 16);
      u32x4 a = *(const u32x4*)&L.xn[c & 3][72 * w + (s < 2 ? 32 * s + 8 * (lane >> 4) : 64)];
#pragma unroll
      for (int k = 0; k < 4; ++k) a[k] = av ? a[k] : 0u;
      u32x4 bb;
#pragma unroll
      for (int k = 0; k < 4; ++k) bb[k] = (unsigned)bw[s][2 * k] | ((unsigned)bw[s][2 * k + 1] << 16);
      acc = mfma16(a, bb, acc);
    }
    if (lane < 16) {
#pragma unroll
      for (int bb = 0; bb < 4; ++bb) L.red[w][bb * 16 + lane] = acc[bb];
    }
  }
  asm volatile("s_waitcnt vmcnt(8)" ::: "memory");
  STAMP(2);
  __syncthreads();
  const u64 e1 = (((u64)ev[1] << 32) | ev[0]) + 1ull;
  const unsigned tag32 = (unsigned)e1 | 0x80000000u;
  const unsigned tq_lo = ((unsigned)e1) << 16, tq_hi = (unsigned)(e1 >> 16);
  if (w == 0) {
    float s = 0.f;
#pragma unroll
    for (int ww = 0; ww < NW; ++ww) s += L.red[ww][lane];
    const u32x2 g = {tq_lo | (cvtpk(s, 0.f) & 0xffffu), tq_hi};
    __builtin_amdgcn_raw_buffer_store_b64(g, rs, WS_Q0 + (((lane >> 4) * HKV + kvh) * 256 + 16 * gi + c) * 8, 0, AUX_SC1);
  }
  STAMP(3);
  SB();
  float hv[9], ya[9];
#pragma unroll
  for (int u = 0; u < 9; ++u) { hv[u] = bflo(cvtpk((bf2f(L.xn[bo][d0 + u]) - L.st2[bo][0]) * L.st2[bo][1], 0.f)); ya[u] = 0.f; }
  constexpr float G0 = -2.0f * 0.7978845608028654f * 1.4426950408889634f, G1 = G0 * 0.044715f;
#define MLP_STEP(a1_, c1_, a2_) do { const float A1 = (a1_), C1 = (c1_), A2 = (a2_); \
    _Pragma("unroll") for (int u = 0; u < 9; ++u) { \
      const float uu = fmaf(hv[u], A1, C1); const float t = fmaf(uu * uu, G1, G0); \
      const float e = __builtin_amdgcn_exp2f(uu * t); const float gl = uu * __builtin_amdgcn_rcpf(1.0f + e); \
      ya[u] = fmaf(gl, A2, ya[u]); } } while (0)
#define PIN_YA() do { _Pragma("unroll") for (int u = 0; u < 9; ++u) asm volatile("" : "+v"(ya[u])); SB(); } while (0)
  MLP_STEP(bf2f(m1s), bf2f(mbs), bf2f(m2s));
  PIN_YA();
#pragma unroll
  for (int k = 0; k < 4; ++k) {
    if (k + 1 < DEPTH) { ISSUE_K(k + 1, k + 1); }
    SB();
    MLP_STEP(bflo(m1v[k]), bflo(mbv[k]), bflo(m2v[k]));
    PIN_YA();
    if (k + 1 < DEPTH) { ISSUE_V(k + 1, k + 1); }
    SB();
    MLP_STEP(bfhi(m1v[k]), bfhi(mbv[k]), bfhi(m2v[k]));
    PIN_YA();
  }
#pragma unroll
  for (int u = 0; u < 9; ++u) { const float s = wave_sum(ya[u]); if (lane == 0) L.red2[w][u] = s; }
  STAMP(4);
  if (w == 0) {
    const int qoff = WS_Q0 + ((b * HKV + kvh) * 256) * 8 + lane * 32;
    u32x4 g0, g1;
    for (unsigned spins = 0; spins < SPIN_CAP; ++spins) {
      asm volatile("" ::: "memory");
      g0 = __builtin_amdgcn_raw_buffer_load_b128(rs, qoff, 0, AUX_SC1);
      g1 = __builtin_amdgcn_raw_buffer_load_b128(rs, qoff + 16, 0, AUX_SC1);
      const unsigned bad = ((g0[0] ^ tq_lo) >> 16) | (g0[1] ^ tq_hi) | ((g0[2] ^ tq_lo) >> 16) | (g0[3] ^ tq_hi) |
                           ((g1[0] ^ tq_lo) >> 16) | (g1[1] ^ tq_hi) | ((g1[2] ^ tq_lo) >> 16) | (g1[3] ^ tq_hi);
      if (__all(bad == 0)) break;
    }
    const u32x2 qq = {(g0[0] & 0xffffu) | (g0[2] << 16), (g1[0] & 0xffffu) | (g1[2] << 16)};
    *(u32x2*)&L.q[4 * lane] = qq;
  }
  STAMP(5);
  __syncthreads();
  const u32x4 qa = *(const u32x4*)&L.q[hl * 128 + 8 * c];
  const u32x4 qb = *(const u32x4*)&L.q[(1 - hl) * 128 + 8 * c];
  unsigned msk[4];
#pragma unroll
  for (int k = 0; k < 4; ++k) msk[k] = ((c & 7) >> 1) == k ? ((c & 1) ? 0xffff0000u : 0x0000ffffu) : 0u;
  f32x4 acc = {0.f, 0.f, 0.f, 0.f};
  float m = -INFINITY, l = 0.f;

#define TILE_QK(sl) \
    float s_[4]; \
    _Pragma("unroll") for (int t = 0; t < 4; ++t) { \
      float xa = 0.f, xb = 0.f; \
      _Pragma("unroll") for (int k = 0; k < 4; ++k) { xa = dot2(Kr[sl][t][k], qa[k], xa); xb = dot2(Kr[sl][t][k], qb[k], xb); } \
      float y = xa + dpp<ROR8>(xb); \
      y += dpp<XOR7>(y); y += dpp<XOR1>(y); y += dpp<XOR2>(y); \
      s_[t] = y; }
#define TILE_PV(sl) \
    { float mt = fmaxf(fmaxf(s_[0], s_[1]), fmaxf(s_[2], s_[3])); \
    mt = xrow_max(mt); \
    const float mn = fmaxf(m, mt); \
    const float alpha = __builtin_amdgcn_exp2f((m - mn) * C2); \
    m = mn; \
    const float mc = -mn * C2; \
    acc *= alpha; l *= alpha; \
    _Pragma("unroll") for (int t = 0; t < 4; ++t) { \
      const float p = __builtin_amdgcn_exp2f(fmaf(s_[t], C2, mc)); \
      l += p; \
      const unsigned pp = cvtpk(p, p); \
      const u32x4 bb = {pp & msk[0], pp & msk[1], pp & msk[2], pp & msk[3]}; \
      acc = mfma16(Vr[sl][t], bb, acc); } }

  unsigned short wov[4][8];
  const int n = 16 * w + c;
  const GAS unsigned short* wop = (const GAS unsigned short*)wo + (size_t)(128 * h + 8 * (lane >> 4)) * DM + 72 * cg + (n < 72 ? n : 71);
#pragma unroll
  for (int it = 0; it < NT; ++it) {
    const int sl = it % DEPTH;
    TILE_QK(sl)
    if (it + DEPTH < NT) { ISSUE_K(sl, it + DEPTH); }
    SB();
    TILE_PV(sl)
    if (it + DEPTH < NT) { ISSUE_V(sl, it + DEPTH); }
    else {
#pragma unroll
      for (int ks = 0; ks < 4; ++ks)
        if (NT - DEPTH + ks * DEPTH / 4 == it) {
#pragma unroll
          for (int j = 0; j < 8; ++j) wov[ks][j] = wop[(size_t)(32 * ks + j) * DM];
        }
    }
    SB();
  }
  STAMP(6);
  l = xrow_sum(l);
#pragma unroll
  for (int i = 0; i < 4; ++i) L.mrg[w][4 + hl * 128 + 8 * (4 * (lane >> 4) + i) + (lane & 7)] = acc[i];
  if ((lane & 55) == 0) { L.mrg[w][hl * 2] = m; L.mrg[w][hl * 2 + 1] = l; }
  __syncthreads();
  if (tid < 256) {
    const int hh = tid >> 7, d = tid & 127;
    float M = -INFINITY;
#pragma unroll
    for (int ww = 0; ww < NW; ++ww) M = fmaxf(M, L.mrg[ww][hh * 2]);
    float Ls = 0.f, o = 0.f;
#pragma unroll
    for (int ww = 0; ww < NW; ++ww) {
      const float wt = __builtin_amdgcn_exp2f((L.mrg[ww][hh * 2] - M) * C2);
      Ls = fmaf(wt, L.mrg[ww][hh * 2 + 1], Ls);
      o = fmaf(wt, L.mrg[ww][4 + tid], o);
    }
    const int slot = ((2 * kvh + hh) * NB + b) * NSPLIT + split;
    const u32x2 g = {__float_as_uint(o), tag32};
    __builtin_amdgcn_raw_buffer_store_b64(g, rs, WS_P1A + slot * 1024 + d * 8, 0, AUX_SC1);
    if (d < 2) {
      const u32x2 gm = {__float_as_uint(d == 0 ? M : Ls), tag32};
      __builtin_amdgcn_raw_buffer_store_b64(gm, rs, WS_P1M + slot * 16 + d * 8, 0, AUX_SC1);
    }
  }
  STAMP(7);
  {
    const int bb = tid >> 7, d = tid & 127;
    const int sbase = (h * NB + bb) * NSPLIT;
    u32x2 ga[NSPLIT]; u32x4 gm[NSPLIT];
    for (unsigned spins = 0; spins < SPIN_CAP; ++spins) {
      asm volatile("" ::: "memory");
#pragma unroll
      for (int s = 0; s < NSPLIT; ++s) {
        ga[s] = __builtin_amdgcn_raw_buffer_load_b64(rs, WS_P1A + (sbase + s) * 1024 + d * 8, 0, AUX_SC1);
        gm[s] = __builtin_amdgcn_raw_buffer_load_b128(rs, WS_P1M + (sbase + s) * 16, 0, AUX_SC1);
      }
      unsigned bad = 0;
#pragma unroll
      for (int s = 0; s < NSPLIT; ++s) bad |= (ga[s][1] ^ tag32) | (gm[s][1] ^ tag32) | (gm[s][3] ^ tag32);
      if (__all(bad == 0)) break;
      __builtin_amdgcn_s_sleep(4);
    }
    float M = -INFINITY;
#pragma unroll
    for (int s = 0; s < NSPLIT; ++s) M = fmaxf(M, __uint_as_float(gm[s][0]));
    float Ls = 0.f, o = 0.f;
#pragma unroll
    for (int s = 0; s < NSPLIT; ++s) {
      const float wt = __builtin_amdgcn_exp2f((__uint_as_float(gm[s][0]) - M) * C2);
      Ls = fmaf(wt, __uint_as_float(gm[s][2]), Ls);
      o = fmaf(wt, __uint_as_float(ga[s][0]), o);
    }
    L.o[bb][d] = (unsigned short)(cvtpk(o / Ls, 0.f) & 0xffffu);
  }
  STAMP(8);
  __syncthreads();
  if (w < 5) {
    f32x4 a2 = {0.f, 0.f, 0.f, 0.f};
#pragma unroll
    for (int ks = 0; ks < 4; ++ks) {
      u32x4 a = {0, 0, 0, 0};
      if (c < 4) a = *(const u32x4*)&L.o[c][32 * ks + 8 * (lane >> 4)];
      u32x4 bb;
#pragma unroll
      for (int k = 0; k < 4; ++k) bb[k] = (unsigned)wov[ks][2 * k] | ((unsigned)wov[ks][2 * k + 1] << 16);
      a2 = mfma16(a, bb, a2);
    }
    if (lane < 16 && n < 72) {
#pragma unroll
      for (int bb = 0; bb < 4; ++bb) {
        const u32x2 g = {__float_as_uint(a2[bb]), tag32};
        __builtin_amdgcn_raw_buffer_store_b64(g, rs, WS_P2 + ((cg * HQ + h) * 288 + bb * 72 + n) * 8, 0, AUX_SC1);
      }
    }
  }
  STAMP(9);
  if (tid < 320) {
    const int u = tid >> 5, hh = tid & 31;
    const bool act = u < 9;
    const int off = WS_P2 + ((cg * HQ + hh) * 288 + bo * 72 + 9 * (h & 7) + (act ? u : 0)) * 8;
    u32x2 g;
    for (unsigned spins = 0; spins < SPIN_CAP; ++spins) {
      asm volatile("" ::: "memory");
      g = __builtin_amdgcn_raw_buffer_load_b64(rs, off, 0, AUX_SC1);
      if (__all(g[1] == tag32)) break;
      __builtin_amdgcn_s_sleep(2);
    }
    float s = x16_sum(row_sum(__uint_as_float(g[0])));
    if (act && hh == 0) {
      float y = bf2f(b2[0]);
#pragma unroll
      for (int ww = 0; ww < NW; ++ww) y += L.red2[ww][u];
      const float gate = y / (1.0f + __expf(-y));
      out[bo * DM + d0 + u] = (unsigned short)(cvtpk(bf2f(x[bo * DM + d0 + u]) + s * gate, 0.f) & 0xffffu);
    }
    if (jb == 0 && tid == 0) {
      const u32x2 en = {(unsigned)e1, (unsigned)(e1 >> 32)};
      __builtin_amdgcn_raw_buffer_store_b64(en, rs, WS_E, 0, AUX_SC1);
    }
  }
  STAMP(10);
}

extern "C" void kernel_launch(void* const* d_in, const int* in_sizes, int n_in, void* d_out, int out_size, void* d_ws, size_t ws_size,
                              hipStream_t stream) {
  k_fused<<<NWG, NTHR, 0, stream>>>((const unsigned short*)d_in[0], (const unsigned short*)d_in[1], (const unsigned short*)d_in[2],
                                    (const unsigned short*)d_in[3], (const unsigned short*)d_in[4], (const unsigned short*)d_in[6],
                                    (const unsigned short*)d_in[7], (const unsigned short*)d_in[8], (const unsigned short*)d_in[9],
                                    (unsigned char*)d_ws, (unsigned short*)d_out);
}
```

```cpp
#include <hip/hip_runtime.h>
#include <hip/hip_bf16.h>
#include <stdint.h>
#pragma clang diagnostic ignored "-Winline-asm"

#define GAS __attribute__((address_space(1)))
typedef unsigned u32x4 __attribute__((ext_vector_type(4)));
typedef unsigned u32x2 __attribute__((ext_vector_type(2)));
typedef float f32x4 __attribute__((ext_vector_type(4)));
typedef float f32x2 __attribute__((ext_vector_type(2)));
typedef short bf16x8 __attribute__((ext_vector_type(8)));
typedef __bf16 bf16x2 __attribute__((ext_vector_type(2)));
typedef unsigned long long u64;

constexpr int NB = 4, DM = 576, HQ = 32, HKV = 16, SK = 8192, DH = 128, FF = 4608;
constexpr int NWG = 256, NW = 8, NTHR = 512;
#define DEPTH 4
#define PACE 2
#define NL 3
#define ROT 4
#define QSLEEP 3
#define QRETRY 8
#define H1RETRY 4
#define H2RETRY 4
#define H2GATE 4
#define STAG 0
#define STAG0 8
constexpr int NSPLIT = 4, KEYS_WG = SK / NSPLIT;
constexpr int NT = KEYS_WG / (16 * NW);
constexpr float C2 = 0.088388347648318440f * 1.4426950408889634f;
constexpr unsigned SPIN_CAP = 1u << 16;

constexpr int WS_E = 0;
constexpr int WS_FL = 4096;
constexpr int WS_Q0 = 8192;
constexpr int WS_P1A = WS_Q0 + NB * HKV * 4 * 256 * 8;
constexpr int WS_P2 = WS_P1A + HQ * NB * NSPLIT * 128 * 8;
constexpr int WS_END = WS_P2 + 8 * HQ * NB * 72 * 4;
constexpr int WS_STAMP = 4 << 20;
constexpr int AUX_SC1 = 16;

__device__ __forceinline__ float bf2f(unsigned short v) { return __builtin_bit_cast(float, (unsigned)v << 16); }
__device__ __forceinline__ float bflo(unsigned v) { return __builtin_bit_cast(float, v << 16); }
__device__ __forceinline__ float bfhi(unsigned v) { return __builtin_bit_cast(float, v & 0xffff0000u); }
__device__ __forceinline__ unsigned cvtpk(float lo, float hi) {
  bf16x2 r = {(__bf16)lo, (__bf16)hi};
  return __builtin_bit_cast(unsigned, r);
}
__device__ __forceinline__ float dot2(unsigned k, unsigned q, float acc) {
  return __builtin_amdgcn_fdot2_f32_bf16(__builtin_bit_cast(bf16x2, k), __builtin_bit_cast(bf16x2, q), acc, false);
}
template <int CTRL> __device__ __forceinline__ float dpp(float x) {
  return __builtin_bit_cast(float, __builtin_amdgcn_mov_dpp(__builtin_bit_cast(int, x), CTRL, 0xf, 0xf, true));
}
constexpr int XOR1 = 0xB1, XOR2 = 0x4E, XOR7 = 0x141, ROR8 = 0x128;
__device__ __forceinline__ float x16_sum(float x) {
  auto s = __builtin_amdgcn_permlane16_swap(__float_as_uint(x), __float_as_uint(x), false, false);
  return __uint_as_float(s[0]) + __uint_as_float(s[1]);
}
__device__ __forceinline__ float xrow_max(float x) {
  auto s = __builtin_amdgcn_permlane16_swap(__float_as_uint(x), __float_as_uint(x), false, false);
  x = fmaxf(__uint_as_float(s[0]), __uint_as_float(s[1]));
  auto t = __builtin_amdgcn_permlane32_swap(__float_as_uint(x), __float_as_uint(x), false, false);
  return fmaxf(__uint_as_float(t[0]), __uint_as_float(t[1]));
}
__device__ __forceinline__ float xrow_sum(float x) {
  x = x16_sum(x);
  auto t = __builtin_amdgcn_permlane32_swap(__float_as_uint(x), __float_as_uint(x), false, false);
  return __uint_as_float(t[0]) + __uint_as_float(t[1]);
}
__device__ __forceinline__ float row_sum(float x) {
  x += dpp<XOR1>(x); x += dpp<XOR2>(x); x += dpp<XOR7>(x); x += dpp<ROR8>(x);
  return x;
}
__device__ __forceinline__ float wave_sum(float x) { return xrow_sum(row_sum(x)); }
__device__ __forceinline__ f32x4 mfma16(u32x4 a, u32x4 b, f32x4 c) {
  asm volatile("s_nop 1\n\tv_mfma_f32_16x16x32_bf16 %0, %1, %2, %0\n\ts_nop 7" : "+v"(c) : "v"(a), "v"(b));
  return c;
}
__device__ __forceinline__ u32x4 ldnt(const GAS u32x4* p) { return __builtin_nontemporal_load(p); }
#define SB() do { asm volatile("" ::: "memory"); __builtin_amdgcn_sched_barrier(0); } while (0)
#define ORDER() __builtin_amdgcn_fence(__ATOMIC_ACQUIRE, "wavefront")

#define STAMP(k) do {} while (0)
#define LAS __attribute__((address_space(3)))
#define RING_CLOBBERS "a0", "a1", "a2", "a3", "a4", "a5", "a6", "a7", "a8", "a9", "a10", "a11", "a12", "a13", "a14", "a15", "a16", "a17", "a18", "a19", "a20", "a21", "a22", "a23", "a24", "a25", "a26", "a27", "a28", "a29", "a30", "a31", "a32", "a33", "a34", "a35", "a36", "a37", "a38", "a39", "a40", "a41", "a42", "a43", "a44", "a45", "a46", "a47", "a48", "a49", "a50", "a51", "a52", "a53", "a54", "a55", "a56", "a57", "a58", "a59", "a60", "a61", "a62", "a63", "a64", "a65", "a66", "a67", "a68", "a69", "a70", "a71", "a72", "a73", "a74", "a75", "a76", "a77", "a78", "a79", "a80", "a81", "a82", "a83", "a84", "a85", "a86", "a87", "a88", "a89", "a90", "a91", "a92", "a93", "a94", "a95", "a96", "a97", "a98", "a99", "a100", "a101", "a102", "a103", "a104", "a105", "a106", "a107", "a108", "a109", "a110", "a111", "a112", "a113", "a114", "a115", "a116", "a117", "a118", "a119", "a120", "a121", "a122", "a123", "a124", "a125", "a126", "a127"

struct Lds {
  u32x4 kv[4][NL > 0 ? NL : 1][8][64];
  unsigned char wt[4][5120];
  unsigned short q[256];
  unsigned short o[NB][DH];
  float st1[NB][2];
  float red2[4][12];
  float mrg[4][264];
  __attribute__((aligned(16))) unsigned sy[8];
};
constexpr int NTILES = KEYS_WG / 16;
static_assert(DEPTH <= 4, "the ring lives in a[0:127]");
constexpr int VMW = 4 + 8 * (DEPTH - 1);

template <int SL> __device__ __forceinline__ void ring_fill(u32x4 rk, u32x4 rv, int koff, int voff, int soff) {
  asm volatile(
      "s_nop 4\n\t"
      "buffer_load_dwordx4 a[%c0+0:%c0+3], %1, %3, %5 offen nt\n\t"
      "buffer_load_dwordx4 a[%c0+4:%c0+7], %1, %3, %5 offen offset:64 nt\n\t"
      "buffer_load_dwordx4 a[%c0+8:%c0+11], %1, %3, %5 offen offset:128 nt\n\t"
      "buffer_load_dwordx4 a[%c0+12:%c0+15], %1, %3, %5 offen offset:192 nt\n\t"
      "buffer_load_dwordx4 a[%c0+16:%c0+19], %2, %4, %5 offen nt\n\t"
      "buffer_load_dwordx4 a[%c0+20:%c0+23], %2, %4, %5 offen offset:256 nt\n\t"
      "buffer_load_dwordx4 a[%c0+24:%c0+27], %2, %4, %5 offen offset:512 nt\n\t"
      "buffer_load_dwordx4 a[%c0+28:%c0+31], %2, %4, %5 offen offset:768 nt"
      :: "i"(32 * SL), "v"(koff), "v"(voff), "s"(rk), "s"(rv), "s"(soff) : "memory", RING_CLOBBERS);
}
template <int SL> __device__ __forceinline__ void step_qk(f32x4& S, int& idx, int& soff, const u32x4 (&qf)[4], u32x4 rk, int koff,
                                                          unsigned next_addr, unsigned one, int rot) {
  unsigned g;
  asm volatile(
      "s_mov_b64 exec, 1\n\t"
      "ds_add_rtn_u32 %3, %11, %12\n\t"
      "s_mov_b64 exec, -1\n\t"
      "s_waitcnt vmcnt(%c13)\n\t"
      "v_mfma_f32_16x16x32_bf16 %0, a[%c4+0:%c4+3], %5, 0\n\t"
      "v_mfma_f32_16x16x32_bf16 %0, a[%c4+4:%c4+7], %6, %0\n\t"
      "v_mfma_f32_16x16x32_bf16 %0, a[%c4+8:%c4+11], %7, %0\n\t"
      "v_mfma_f32_16x16x32_bf16 %0, a[%c4+12:%c4+15], %8, %0\n\t"
      "s_waitcnt lgkmcnt(0)\n\t"
      "v_readfirstlane_b32 %1, %3\n\t"
      "s_nop 0\n\t"
      "s_add_u32 %2, %1, %14\n\t"
      "s_and_b32 %2, %2, 127\n\t"
      "s_lshl_b32 %2, %2, 12\n\t"
      "s_cmp_lt_u32 %1, 128\n\t"
      "s_cselect_b32 %2, %2, 0x40000000\n\t"
      "buffer_load_dwordx4 a[%c4+0:%c4+3], %10, %9, %2 offen nt\n\t"
      "buffer_load_dwordx4 a[%c4+4:%c4+7], %10, %9, %2 offen offset:64 nt\n\t"
      "buffer_load_dwordx4 a[%c4+8:%c4+11], %10, %9, %2 offen offset:128 nt\n\t"
      "buffer_load_dwordx4 a[%c4+12:%c4+15], %10, %9, %2 offen offset:192 nt\n\t"
      "s_nop 1"
      : "=&v"(S), "=&s"(idx), "=&s"(soff), "=&v"(g)
      : "i"(32 * SL), "v"(qf[0]), "v"(qf[1]), "v"(qf[2]), "v"(qf[3]), "s"(rk), "v"(koff), "v"(next_addr), "v"(one), "i"(VMW), "s"(rot)
      : "memory", "scc", RING_CLOBBERS);
}
template <int SL> __device__ __forceinline__ void step_pv(f32x4& acc, const u32x4 (&bb)[4], u32x4 rv, int voff, int soff) {
  asm volatile(
      "s_waitcnt vmcnt(%c9)\n\t"
      "s_nop 0\n\t"
      "v_mfma_f32_16x16x32_bf16 %0, a[%c1+16:%c1+19], %2, %0\n\t"
      "v_mfma_f32_16x16x32_bf16 %0, a[%c1+20:%c1+23], %3, %0\n\t"
      "v_mfma_f32_16x16x32_bf16 %0, a[%c1+24:%c1+27], %4, %0\n\t"
      "v_mfma_f32_16x16x32_bf16 %0, a[%c1+28:%c1+31], %5, %0\n\t"
      "buffer_load_dwordx4 a[%c1+16:%c1+19], %7, %6, %8 offen nt\n\t"
      "buffer_load_dwordx4 a[%c1+20:%c1+23], %7, %6, %8 offen offset:256 nt\n\t"
      "buffer_load_dwordx4 a[%c1+24:%c1+27], %7, %6, %8 offen offset:512 nt\n\t"
      "buffer_load_dwordx4 a[%c1+28:%c1+31], %7, %6, %8 offen offset:768 nt\n\t"
      "s_nop 4"
      : "+v"(acc)
      : "i"(32 * SL), "v"(bb[0]), "v"(bb[1]), "v"(bb[2]), "v"(bb[3]), "s"(rv), "v"(voff), "s"(soff), "i"(VMW)
      : "memory", RING_CLOBBERS);
}
__device__ __forceinline__ void lds_fill(u32x4 rk, u32x4 rv, int koff, int voff, int soff, unsigned dst) {
  unsigned t;
  asm volatile(
      "s_mov_b32 m0, %6\n\ts_add_u32 %0, %6, 960\n\t"
      "buffer_load_dwordx4 %1, %3, %5 offen nt lds\n\t"
      "s_mov_b32 m0, %0\n\ts_add_u32 %0, %0, 960\n\t"
      "buffer_load_dwordx4 %1, %3, %5 offen offset:64 nt lds\n\t"
      "s_mov_b32 m0, %0\n\ts_add_u32 %0, %0, 960\n\t"
      "buffer_load_dwordx4 %1, %3, %5 offen offset:128 nt lds\n\t"
      "s_mov_b32 m0, %0\n\ts_add_u32 %0, %6, 4096\n\t"
      "buffer_load_dwordx4 %1, %3, %5 offen offset:192 nt lds\n\t"
      "s_mov_b32 m0, %0\n\ts_add_u32 %0, %0, 768\n\t"
      "buffer_load_dwordx4 %2, %4, %5 offen nt lds\n\t"
      "s_mov_b32 m0, %0\n\ts_add_u32 %0, %0, 768\n\t"
      "buffer_load_dwordx4 %2, %4, %5 offen offset:256 nt lds\n\t"
      "s_mov_b32 m0, %0\n\ts_add_u32 %0, %0, 768\n\t"
      "buffer_load_dwordx4 %2, %4, %5 offen offset:512 nt lds\n\t"
      "s_mov_b32 m0, %0\n\ts_nop 0\n\t"
      "buffer_load_dwordx4 %2, %4, %5 offen offset:768 nt lds"
      : "=&s"(t) : "v"(koff), "v"(voff), "s"(rk), "s"(rv), "s"(soff), "s"(dst) : "memory", "scc", "m0");
}
__device__ __forceinline__ void dma16(const void* g, unsigned dst) {
  asm volatile("s_mov_b32 m0, %1\n\ts_nop 0\n\tglobal_load_lds_dwordx4 %0, off nt" :: "v"(g), "s"(dst) : "memory", "m0");
}
__device__ __forceinline__ void mfma4(f32x4& d, const u32x4 (&a)[4], const u32x4 (&b)[4]) {
  asm volatile("s_nop 1\n\t"
               "v_mfma_f32_16x16x32_bf16 %0, %1, %5, %0\n\tv_mfma_f32_16x16x32_bf16 %0, %2, %6, %0\n\t"
               "v_mfma_f32_16x16x32_bf16 %0, %3, %7, %0\n\tv_mfma_f32_16x16x32_bf16 %0, %4, %8, %0\n\ts_nop 7"
               : "+v"(d) : "v"(a[0]), "v"(a[1]), "v"(a[2]), "v"(a[3]), "v"(b[0]), "v"(b[1]), "v"(b[2]), "v"(b[3]));
}
typedef short s16x4 __attribute__((ext_vector_type(4)));
__device__ __forceinline__ u32x4 tr_frag(const unsigned char* p, int step) {
  const u32x2 lo = __builtin_bit_cast(u32x2, __builtin_amdgcn_ds_read_tr16_b64_v4i16((LAS s16x4*)p));
  const u32x2 hi = __builtin_bit_cast(u32x2, __builtin_amdgcn_ds_read_tr16_b64_v4i16((LAS s16x4*)(p + step)));
  return u32x4{lo[0], lo[1], hi[0], hi[1]};
}
__device__ __forceinline__ u32x4 make_rsrc(const void* p, unsigned bytes) {
  const u64 a = (u64)p;
  u32x4 r = {(unsigned)__builtin_amdgcn_readfirstlane((unsigned)a), (unsigned)__builtin_amdgcn_readfirstlane((unsigned)(a >> 32) & 0xffffu), bytes, 0x00020000u};
  return r;
}
#define NTS ""
#define LD16(dst, p, OFF) asm volatile("global_load_dwordx4 %0, %1, off offset:%c2" NTS : "=v"(dst) : "v"(p), "i"(OFF) : "memory")
#define LDU16(dst, p, OFF) asm volatile("global_load_ushort %0, %1, off offset:%c2" NTS : "=v"(dst) : "v"(p), "i"(OFF) : "memory")
#define LD4(dst, p, OFF) asm volatile("global_load_dword %0, %1, off offset:%c2" NTS : "=v"(dst) : "v"(p), "i"(OFF) : "memory")
#define TIE8(a) asm volatile("" : "+v"(a[0]), "+v"(a[1]), "+v"(a[2]), "+v"(a[3]), "+v"(a[4]), "+v"(a[5]), "+v"(a[6]), "+v"(a[7]))

__device__ __forceinline__ unsigned lds_peek(unsigned addr) {
  unsigned f;
  asm volatile("ds_read_b32 %0, %1\n\ts_waitcnt lgkmcnt(0)" : "=v"(f) : "v"(addr) : "memory");
  return __builtin_amdgcn_readfirstlane(f);
}
__device__ __forceinline__ void lds_arrive(unsigned addr, unsigned one) {
  asm volatile("s_mov_b64 exec, 1\n\tds_add_u32 %0, %1\n\ts_mov_b64 exec, -1" :: "v"(addr), "v"(one) : "memory");
}

__global__ void __launch_bounds__(NTHR, 2)
k_fused(const unsigned short* __restrict__ x, const unsigned short* __restrict__ kc, const unsigned short* __restrict__ vc,
        const unsigned short* __restrict__ wq, const unsigned short* __restrict__ wo, const unsigned short* __restrict__ w1,
        const unsigned short* __restrict__ b1, const unsigned short* __restrict__ w2, const unsigned short* __restrict__ b2,
        unsigned char* ws, unsigned short* __restrict__ out) {
  __shared__ __attribute__((aligned(16))) Lds L;
  const int tid = threadIdx.x, lane = tid & 63, w = __builtin_amdgcn_readfirstlane(tid >> 6);
  STAMP(0);
  if (tid == 0) {
    *(u32x4*)&L.sy[0] = u32x4{4u, 0u, 0u, 0u};
    *(u32x4*)&L.sy[4] = u32x4{0u, 0u, 0u, 0u};
  }
  asm volatile("s_waitcnt lgkmcnt(0)\n\ts_barrier" :: "s"(x), "s"(kc), "s"(vc), "s"(wq), "s"(ws) : "memory");
  const int jb = blockIdx.x;
  const int kvh = 2 * (jb & 7) + ((jb >> 3) & 1), gi = jb >> 4;
  const int b = gi >> 2, split = gi & 3;
  const int h = 2 * kvh + (gi >> 3), cg = gi & 7;
  const int bo = h >> 3, d0 = 72 * cg + 9 * (h & 7);
  const int c = lane & 15, hl = c >> 3, g4 = lane >> 4;
  const __amdgpu_buffer_rsrc_t rs = __builtin_amdgcn_make_buffer_rsrc((void*)ws, 0, WS_END, 0x00020000);
  const unsigned sy_addr = (unsigned)(size_t)(LAS unsigned*)&L.sy[0];
  const unsigned one = 1u;
  const bool xact = lane < 36;
#define UNPACK16(f, v0, v1) do { _Pragma("unroll") for (int k = 0; k < 4; ++k) { \
    f[2 * k] = bflo(v0[k]); f[2 * k + 1] = bfhi(v0[k]); f[8 + 2 * k] = bflo(v1[k]); f[8 + 2 * k + 1] = bfhi(v1[k]); } } while (0)
#define LN_STATS(f, mean, rstd) do { float s_ = 0.f; _Pragma("unroll") for (int k = 0; k < 16; ++k) s_ += f[k]; \
    mean = wave_sum(xact ? s_ : 0.f) * (1.f / DM); float q_ = 0.f; \
    _Pragma("unroll") for (int k = 0; k < 16; ++k) { f[k] -= mean; q_ = fmaf(f[k], f[k], q_); } \
    rstd = __builtin_amdgcn_rsqf(wave_sum(xact ? q_ : 0.f) * (1.f / DM) + 1e-5f); } while (0)

  if (__builtin_expect(w < 4, 1)) {
    __builtin_amdgcn_s_setprio(3);
    const unsigned wt_addr = (unsigned)(size_t)(LAS unsigned char*)&L.wt[w][0];
    u32x2 e1;
    {
      u32x4 xa[5], xv0, xv1;
      u32x2 ev;
      const int r0 = 144 * (gi >> 2);
      {
        const unsigned short* xr = x + w * DM + 8 * (xact ? lane : 0);
        LD16(xv0, xr, 0); LD16(xv1, xr, 576);
        const unsigned short* wp = wq + (size_t)(r0 + 36 * w + (lane >> 3)) * 4096 + 256 * kvh + 64 * (gi & 3) + 8 * (lane & 7);
        const unsigned short* wp2 = wq + (size_t)(r0 + 36 * w + 32 + ((lane >> 3) & 3)) * 4096 + 256 * kvh + 64 * (gi & 3) + 8 * (lane & 7);
        dma16(wp, wt_addr); dma16(wp + 8 * 4096, wt_addr + 1024); dma16(wp + 16 * 4096, wt_addr + 2048);
        dma16(wp + 24 * 4096, wt_addr + 3072); dma16(wp2, wt_addr + 4096);
        const unsigned short* xp = x + (c >> 2) * DM + r0 + 8 * g4;
        const unsigned short* xp2 = x + (c >> 2) * DM + r0 + 36 * g4 + 32;
        LD16(xa[0], xp, 0); LD16(xa[1], xp, 72); LD16(xa[2], xp, 144); LD16(xa[3], xp, 216);
        asm volatile("global_load_dwordx2 %0, %1, off" : "=v"(*(u32x2*)&xa[4]) : "v"(xp2) : "memory");
        asm volatile("global_load_dwordx2 %0, %1, off sc1" : "=v"(ev) : "v"(ws + WS_E) : "memory");
      }
      STAMP(1);
      asm volatile("s_waitcnt vmcnt(11)" : "+v"(xv0), "+v"(xv1) :: "memory");
      {
        float f[16], mean, rstd;
        UNPACK16(f, xv0, xv1);
        LN_STATS(f, mean, rstd);
        if (lane == 0) *(f32x2*)&L.st1[w][0] = f32x2{mean, rstd};
      }
      asm volatile("s_waitcnt vmcnt(0)" : "+v"(xa[0]), "+v"(xa[1]), "+v"(xa[2]), "+v"(xa[3]), "+v"(*(u32x2*)&xa[4]), "+v"(ev) :: "memory");
      lds_arrive(sy_addr + 4, one);
      STAMP(2);
      u32x4 a[4], a4;
#pragma unroll
      for (int s = 0; s < 4; ++s)
#pragma unroll
        for (int k = 0; k < 4; ++k) a[s][k] = (c & 3) == 0 ? xa[s][k] : ((c & 3) == 1 ? 0x3F803F80u : 0u);
#pragma unroll
      for (int k = 0; k < 4; ++k) a4[k] = k >= 2 ? 0u : (c & 3) == 0 ? xa[4][k] : ((c & 3) == 1 ? 0x3F803F80u : 0u);
      const u64 e1w = (((u64)ev[1] << 32) | ev[0]) + 1ull;
      e1 = u32x2{(unsigned)e1w, (unsigned)(e1w >> 32)};
      const unsigned tq_lo = e1[0] & 0xffu, tq_hi = (unsigned)(e1w >> 8);
      const int po = WS_Q0 + (((g4 * HKV + kvh) * 4 + (gi >> 2)) * 256 + 64 * (gi & 3) + 16 * w + c) * 8;
      const int qoff = WS_Q0 + ((b * HKV + kvh) * 1024 + 64 * w + lane) * 8;
      asm volatile("" : "+v"(a[0]), "+v"(a[1]), "+v"(a[2]), "+v"(a[3]), "+v"(a4) : "v"(po), "v"(qoff), "v"(tq_lo), "v"(tq_hi));
      while (lds_peek(sy_addr + 4) != 4u) {}
      f32x4 acc = {0.f, 0.f, 0.f, 0.f};
      {
        const unsigned char* tp = &L.wt[0][(8 * g4 + (c >> 2)) * 128 + 32 * w + (c & 3) * 8];
        const unsigned char* tp2 = &L.wt[g4][(32 + (c >> 2)) * 128 + 32 * w + (c & 3) * 8];
        u32x4 bb[4];
#pragma unroll
        for (int s = 0; s < 4; ++s) bb[s] = tr_frag(tp + 5120 * s, 512);
        const u32x2 lo = __builtin_bit_cast(u32x2, __builtin_amdgcn_ds_read_tr16_b64_v4i16((LAS s16x4*)tp2));
        mfma4(acc, a, bb);
        acc = mfma16(a4, u32x4{lo[0], lo[1], 0u, 0u}, acc);
      }
      const f32x2 st = *(const f32x2*)&L.st1[g4][0];
      const float qv = (acc[0] - st[0] * acc[1]) * st[1];
      __builtin_amdgcn_raw_buffer_store_b64(u32x2{((__float_as_uint(qv) + 0x80u) & 0xffffff00u) | tq_lo, tq_hi}, rs, po, 0, AUX_SC1);
      STAMP(3);
      u32x2 g[4];
      for (int i = 0; i < QSLEEP; ++i) __builtin_amdgcn_s_sleep(16);
#pragma nounroll
      for (unsigned spins = 0; spins < SPIN_CAP; ++spins) {
        asm volatile("" ::: "memory");
#pragma unroll
        for (int k = 0; k < 4; ++k) g[k] = __builtin_amdgcn_raw_buffer_load_b64(rs, qoff + 2048 * k, 0, AUX_SC1);
        unsigned bad = 0;
#pragma unroll
        for (int k = 0; k < 4; ++k) bad |= ((g[k][0] ^ tq_lo) & 0xffu) | (g[k][1] ^ tq_hi);
        if (__all(bad == 0)) break;
        __builtin_amdgcn_s_sleep(QRETRY);
      }
      float qs = 0.f;
#pragma unroll
      for (int k = 0; k < 4; ++k) qs += __uint_as_float(g[k][0] & 0xffffff00u);
      L.q[64 * w + lane] = (unsigned short)(cvtpk(qs, 0.f) & 0xffffu);
      lds_arrive(sy_addr + 8, one);
      STAMP(4);
    }
    __builtin_amdgcn_s_setprio(0);
    if (w == 0) *(u32x2*)&L.sy[6] = e1;
    for (int i = 0; i < STAG0 + STAG * (jb >> 3); ++i) __builtin_amdgcn_s_sleep(16);
    while (lds_peek(sy_addr + 8) != 4u) __builtin_amdgcn_s_sleep(4);
    __builtin_amdgcn_s_setprio(3);
    {
      const u32x4 z = {0u, 0u, 0u, 0u};
      if (w < 2) {
        __builtin_amdgcn_raw_buffer_store_b128(z, rs, WS_P1A + (((2 * kvh + w) * NB + b) * NSPLIT + split) * 1024 + lane * 16, 0, AUX_SC1);
      } else {
        __builtin_amdgcn_raw_buffer_store_b128(z, rs, WS_P2 + (cg * HQ + h) * 1152 + (w - 2) * 128 + lane * 16, 0, AUX_SC1);
      }
    }
    {
      u32x4 xv0, xv1, m1v[2], mbv[2], m2v[2];
      unsigned m1s, mbs, m2s, xs;
      {
        const unsigned short* xr = x + bo * DM + 8 * (xact ? lane : 0);
        LD16(xv0, xr, 0); LD16(xv1, xr, 576);
        LDU16(xs, x + bo * DM + d0 + (lane < 9 ? lane : 0), 0);
        LD16(m1v[0], w1 + 8 * tid, 0); LD16(mbv[0], b1 + 8 * tid, 0); LD16(m2v[0], w2 + 8 * tid, 0);
        LD16(m1v[1], w1 + 2048 + 8 * tid, 0); LD16(mbv[1], b1 + 2048 + 8 * tid, 0); LD16(m2v[1], w2 + 2048 + 8 * tid, 0);
        LD4(m1s, w1 + 4096 + 2 * tid, 0); LD4(mbs, b1 + 4096 + 2 * tid, 0); LD4(m2s, w2 + 4096 + 2 * tid, 0);
      }
      {
        const unsigned short* wop = wo + (size_t)(128 * h + 32 * w) * DM + 72 * cg;
#pragma unroll
        for (int t = 0; t < 5; ++t) {
          const int i = t < 4 ? 64 * t + lane : 256 + (lane & 31);
          const int row = (i * 7282) >> 16;
          dma16(wop + row * DM + 8 * (i - 9 * row), wt_addr + 1024 * t);
        }
      }
      __builtin_amdgcn_s_setprio(0);
      asm volatile("s_waitcnt vmcnt(5)" : "+v"(xv0), "+v"(xv1), "+v"(xs), "+v"(m1v[0]), "+v"(mbv[0]), "+v"(m2v[0]), "+v"(m1v[1]), "+v"(mbv[1]), "+v"(m2v[1]) :: "memory");
      asm volatile("" : "+v"(m1s), "+v"(mbs), "+v"(m2s));
      STAMP(5);
      float hv[9];
      {
        float f[16], mean, rstd, mean2, rstd2;
        UNPACK16(f, xv0, xv1);
        LN_STATS(f, mean, rstd);
        u32x4 o0, o1;
#pragma unroll
        for (int k = 0; k < 4; ++k) { o0[k] = cvtpk(f[2 * k] * rstd, f[2 * k + 1] * rstd); o1[k] = cvtpk(f[8 + 2 * k] * rstd, f[8 + 2 * k + 1] * rstd); }
        UNPACK16(f, o0, o1);
        LN_STATS(f, mean2, rstd2);
        const float xn = bflo(cvtpk((bflo(xs) - mean) * rstd, 0.f));
        const unsigned hb = cvtpk(0.f, (xn - mean2) * rstd2);
#pragma unroll
        for (int u = 0; u < 9; ++u) hv[u] = __builtin_bit_cast(float, __builtin_amdgcn_readlane(hb, u));
      }
      constexpr float G0 = -2.0f * 0.7978845608028654f * 1.4426950408889634f, G1 = G0 * 0.044715f;
      f32x2 ya[9];
#pragma unroll
      for (int u = 0; u < 9; ++u) ya[u] = f32x2{0.f, 0.f};
#define NMS 9
#pragma unroll
      for (int ms = 0; ms < NMS; ++ms) {
        const unsigned r1 = ms < 8 ? m1v[ms >> 2][ms & 3] : m1s, rb = ms < 8 ? mbv[ms >> 2][ms & 3] : mbs, r2 = ms < 8 ? m2v[ms >> 2][ms & 3] : m2s;
        const f32x2 A1 = {bflo(r1), bfhi(r1)}, C1 = {bflo(rb), bfhi(rb)}, A2 = {bflo(r2), bfhi(r2)};
#pragma unroll
        for (int u = 0; u < 9; ++u) {
          const f32x2 uu = A1 * hv[u] + C1;
          const f32x2 z = uu * (uu * uu * G1 + G0);
          const f32x2 e = f32x2{__builtin_amdgcn_exp2f(z[0]), __builtin_amdgcn_exp2f(z[1])} + 1.0f;
          const f32x2 gl = uu * f32x2{__builtin_amdgcn_rcpf(e[0]), __builtin_amdgcn_rcpf(e[1])};
          ya[u] = gl * A2 + ya[u];
        }
      }
#pragma unroll
      for (int u = 0; u < 9; ++u) { const float s = wave_sum(ya[u][0] + ya[u][1]); if (lane == 0) L.red2[w][u] = s; }
      asm volatile("s_waitcnt vmcnt(0)" ::: "memory");
      lds_arrive(sy_addr + 12, one);
      STAMP(6);
    }
    while (lds_peek(sy_addr + 12) != 4u) __builtin_amdgcn_s_sleep(8);
    u32x4 wf[5][4];
#pragma unroll
    for (int nt = 0; nt < 5; ++nt)
#pragma unroll
      for (int ks = 0; ks < 4; ++ks) wf[nt][ks] = tr_frag(&L.wt[ks][(8 * g4 + (c >> 2)) * 144 + (c & 3) * 8 + 32 * nt], 576);
    __builtin_amdgcn_s_setprio(3);
    if (tid == 0) __builtin_amdgcn_raw_buffer_store_b64(e1, rs, WS_FL + jb * 8, 0, AUX_SC1);
    while (lds_peek(sy_addr) < (unsigned)(NTILES - 16)) __builtin_amdgcn_s_sleep(32);
    for (unsigned spins = 0; spins < SPIN_CAP; ++spins) {
      asm volatile("" ::: "memory");
      const u32x2 f = __builtin_amdgcn_raw_buffer_load_b64(rs, WS_FL + tid * 8, 0, AUX_SC1);
      if (__all(f[0] == e1[0] && f[1] == e1[1])) break;
      __builtin_amdgcn_s_sleep(8);
    }
    lds_arrive(sy_addr + 20, one);
    while (lds_peek(sy_addr + 20) < 4u) __builtin_amdgcn_s_sleep(2);
    while (lds_peek(sy_addr + 16) != 4u) __builtin_amdgcn_s_sleep(4);
    STAMP(7);
    {
      const int off = WS_P1A + ((h * NB + w) * NSPLIT) * 1024 + lane * 16;
      u32x4 g[NSPLIT];
      unsigned pend = 15u;
      for (unsigned spins = 0; spins < SPIN_CAP; ++spins) {
        asm volatile("" ::: "memory");
#pragma unroll
        for (int s = 0; s < NSPLIT; ++s)
          if (pend & (1u << s)) g[s] = __builtin_amdgcn_raw_buffer_load_b128(rs, off + s * 1024, 0, AUX_SC1);
#pragma unroll
        for (int s = 0; s < NSPLIT; ++s)
          if ((pend & (1u << s)) && __all(g[s][1] & g[s][3] & 1u)) pend &= ~(1u << s);
        if (!pend) break;
        __builtin_amdgcn_s_sleep(H1RETRY);
      }
      STAMP(8);
      float M = -INFINITY;
#pragma unroll
      for (int s = 0; s < NSPLIT; ++s) M = fmaxf(M, __uint_as_float(g[s][1]));
      float Ls = 0.f, o0 = 0.f, o1 = 0.f;
#pragma unroll
      for (int s = 0; s < NSPLIT; ++s) {
        const float wt = __builtin_amdgcn_exp2f(__uint_as_float(g[s][1]) - M);
        Ls += wt;
        o0 = fmaf(wt, __uint_as_float(g[s][0]), o0);
        o1 = fmaf(wt, __uint_as_float(g[s][2]), o1);
      }
      const float r = __builtin_amdgcn_rcpf(Ls);
      *(unsigned*)&L.o[w][2 * lane] = cvtpk(o0 * r, o1 * r);
    }
    {
      f32x4 a2[5];
#pragma unroll
      for (int nt = 0; nt < 5; ++nt) a2[nt] = f32x4{0.f, 0.f, 0.f, 0.f};
      u32x4 a[4];
#pragma unroll
      for (int ks = 0; ks < 4; ++ks) a[ks] = *(const u32x4*)&L.o[w][32 * ks + 8 * g4];
      asm volatile("s_nop 1\n\t"
#define M5(ks, a) "v_mfma_f32_16x16x32_bf16 %0, " a ", %" #ks ", %0\n\t"
                   "v_mfma_f32_16x16x32_bf16 %0, %5, %9, %0\n\tv_mfma_f32_16x16x32_bf16 %1, %5, %13, %1\n\tv_mfma_f32_16x16x32_bf16 %2, %5, %17, %2\n\t"
                   "v_mfma_f32_16x16x32_bf16 %3, %5, %21, %3\n\tv_mfma_f32_16x16x32_bf16 %4, %5, %25, %4\n\t"
                   "v_mfma_f32_16x16x32_bf16 %0, %6, %10, %0\n\tv_mfma_f32_16x16x32_bf16 %1, %6, %14, %1\n\tv_mfma_f32_16x16x32_bf16 %2, %6, %18, %2\n\t"
                   "v_mfma_f32_16x16x32_bf16 %3, %6, %22, %3\n\tv_mfma_f32_16x16x32_bf16 %4, %6, %26, %4\n\t"
                   "v_mfma_f32_16x16x32_bf16 %0, %7, %11, %0\n\tv_mfma_f32_16x16x32_bf16 %1, %7, %15, %1\n\tv_mfma_f32_16x16x32_bf16 %2, %7, %19, %2\n\t"
                   "v_mfma_f32_16x16x32_bf16 %3, %7, %23, %3\n\tv_mfma_f32_16x16x32_bf16 %4, %7, %27, %4\n\t"
                   "v_mfma_f32_16x16x32_bf16 %0, %8, %12, %0\n\tv_mfma_f32_16x16x32_bf16 %1, %8, %16, %1\n\tv_mfma_f32_16x16x32_bf16 %2, %8, %20, %2\n\t"
                   "v_mfma_f32_16x16x32_bf16 %3, %8, %24, %3\n\tv_mfma_f32_16x16x32_bf16 %4, %8, %28, %4\n\t"
                   "s_nop 7"
                   : "+v"(a2[0]), "+v"(a2[1]), "+v"(a2[2]), "+v"(a2[3]), "+v"(a2[4])
                   : "v"(a[0]), "v"(a[1]), "v"(a[2]), "v"(a[3]),
                     "v"(wf[0][0]), "v"(wf[0][1]), "v"(wf[0][2]), "v"(wf[0][3]), "v"(wf[1][0]), "v"(wf[1][1]), "v"(wf[1][2]), "v"(wf[1][3]),
                     "v"(wf[2][0]), "v"(wf[2][1]), "v"(wf[2][2]), "v"(wf[2][3]), "v"(wf[3][0]), "v"(wf[3][1]), "v"(wf[3][2]), "v"(wf[3][3]),
                     "v"(wf[4][0]), "v"(wf[4][1]), "v"(wf[4][2]), "v"(wf[4][3]));
      const float v0 = g4 == 0 ? a2[0][0] : g4 == 1 ? a2[1][0] : g4 == 2 ? a2[2][0] : a2[3][0];
      const int po = WS_P2 + ((cg * HQ + h) * 288 + w * 72 + lane) * 4;
      __builtin_amdgcn_raw_buffer_store_b32(__float_as_uint(v0) | 1u, rs, po, 0, AUX_SC1);
      if (lane < 8) __builtin_amdgcn_raw_buffer_store_b32(__float_as_uint(a2[4][0]) | 1u, rs, po + 256, 0, AUX_SC1);
      lds_arrive(sy_addr + 20, one);
    }
    STAMP(9);
  } else {
    const int sw = w - 4;
    __builtin_amdgcn_s_setprio(1);
#define REC() do {} while (0)
    const int rot = ROT * (jb >> 3);
#define TOFF(t) ((((t) + rot) & 127) * 4096)
    const size_t kvbase = ((size_t)(b * HKV + kvh) * SK + (size_t)split * KEYS_WG) * DH;
    const u32x4 rk = make_rsrc(kc + kvbase, KEYS_WG * DH * 2), rv = make_rsrc(vc + kvbase, KEYS_WG * DH * 2);
    const int koff = c * 256 + g4 * 16, voff = g4 * 1024 + c * 16;
    const unsigned kv_addr = (unsigned)(size_t)(LAS u32x4*)&L.kv[sw][0][0][0];
    asm volatile("s_nop 4");
    lds_fill(rk, rv, koff, voff, TOFF(sw), kv_addr);
    STAMP(1);
    unsigned msk[4];
#pragma unroll
    for (int k = 0; k < 4; ++k) msk[k] = ((c & 7) >> 1) == k ? ((c & 1) ? 0xffff0000u : 0x0000ffffu) : 0u;
    f32x4 acc = {0.f, 0.f, 0.f, 0.f};
    float m = -INFINITY, mc = 0.f, l = 0.f;
    int tix[DEPTH];
#pragma unroll
    for (int d = 0; d < DEPTH; ++d) tix[d] = 0;
    u32x4 qf[4];
#pragma unroll
    for (int i = 0; i < 4; ++i) qf[i] = u32x4{0u, 0u, 0u, 0u};

#define SOFTMAX(S, bb) do { \
    float mt = fmaxf(fmaxf(S[0], S[1]), fmaxf(S[2], S[3])); \
    if (__any(mt > m)) { \
      mt = xrow_max(mt); \
      const float mn = fmaxf(m, mt); \
      const float alpha = __builtin_amdgcn_exp2f((m - mn) * C2); \
      m = mn; mc = -mn * C2; acc *= alpha; l *= alpha; \
    } \
    _Pragma("unroll") for (int t = 0; t < 4; ++t) { \
      const float p = __builtin_amdgcn_exp2f(fmaf(S[t], C2, mc)); \
      l += p; \
      const unsigned pp = cvtpk(p, p); \
      bb[t] = u32x4{pp & msk[0], pp & msk[1], pp & msk[2], pp & msk[3]}; } } while (0)
#define TILE(SL) do { \
    f32x4 S; int nx_, so_; u32x4 bb[4]; \
    step_qk<SL>(S, nx_, so_, qf, rk, koff, sy_addr, one, rot); \
    SOFTMAX(S, bb); \
    step_pv<SL>(acc, bb, rv, voff, so_); REC(); \
    tix[SL] = nx_; } while (0)
    int nl = 1, phase = 0;
    for (;;) {
      if (tix[0] >= NTILES) break;
      TILE(0);
      if (tix[1] >= NTILES) break;
      TILE(1);
      if (tix[2] >= NTILES) break;
      TILE(2);
      if (tix[3] >= NTILES) break;
      TILE(3);
      phase = __builtin_amdgcn_readfirstlane(phase);
      asm volatile("" : "+s"(phase));
      if (phase < 2) {
        if (phase == 0) {
          STAMP(2);
          for (; nl < NL; ++nl) {
            asm volatile("s_waitcnt vmcnt(%c0)" :: "i"(8 * (PACE - 1)) : "memory");
            if (lds_peek(sy_addr + 8) == 4u) break;
            unsigned g;
            asm volatile("s_mov_b64 exec, 1\n\tds_add_rtn_u32 %0, %1, %2\n\ts_mov_b64 exec, -1\n\ts_waitcnt lgkmcnt(0)"
                         : "=&v"(g) : "v"(sy_addr), "v"(one) : "memory");
            const int so = TOFF(__builtin_amdgcn_readfirstlane(g));
            const unsigned dst = __builtin_amdgcn_readfirstlane(kv_addr + 8192 * nl);
            asm volatile("s_nop 4" :: "s"(so), "s"(dst));
            lds_fill(rk, rv, koff, voff, so, dst);
          }
          STAMP(3);
          while (lds_peek(sy_addr + 8) != 4u) __builtin_amdgcn_s_sleep(1);
          STAMP(4);
#pragma unroll
          for (int i = 0; i < 4; ++i) qf[i] = *(const u32x4*)&L.q[hl * 128 + 32 * i + 8 * g4];
          acc = f32x4{0.f, 0.f, 0.f, 0.f};
          m = -INFINITY; mc = 0.f; l = 0.f;
        } else {
          asm volatile("s_waitcnt vmcnt(%c0)" :: "i"(8 * DEPTH) : "memory");
          for (; nl > 0; --nl) {
            u32x4 kf[4], vf[4], bb[4];
#pragma unroll
            for (int i = 0; i < 4; ++i) { kf[i] = L.kv[sw][nl - 1][i][lane]; vf[i] = L.kv[sw][nl - 1][4 + i][lane]; }
            f32x4 S = {0.f, 0.f, 0.f, 0.f};
            mfma4(S, kf, qf);
            SOFTMAX(S, bb);
            mfma4(acc, vf, bb); REC();
          }
        }
        ++phase;
      }
    }
    STAMP(5);
    l = xrow_sum(l);
#pragma unroll
    for (int i = 0; i < 4; ++i) L.mrg[sw][8 + hl * 128 + 8 * (4 * g4 + i) + (lane & 7)] = acc[i];
    if ((lane & 55) == 0) *(f32x2*)&L.mrg[sw][hl * 2] = f32x2{m, l};
    unsigned arrived;
    asm volatile("s_mov_b64 exec, 1\n\tds_add_rtn_u32 %0, %1, %2 offset:16\n\ts_mov_b64 exec, -1\n\ts_waitcnt lgkmcnt(0)"
                 : "=v"(arrived) : "v"(sy_addr), "v"(one) : "memory");
    if (__builtin_amdgcn_readfirstlane(arrived) == 3u) {
      const int hh = lane >> 5;
      while (lds_peek(sy_addr + 12) != 4u) __builtin_amdgcn_s_sleep(4);
      float M = -INFINITY;
      f32x2 ml[4];
#pragma unroll
      for (int ww = 0; ww < 4; ++ww) { ml[ww] = *(const f32x2*)&L.mrg[ww][hh * 2]; M = fmaxf(M, ml[ww][0]); }
      float Ls = 0.f;
      f32x4 o = {0.f, 0.f, 0.f, 0.f};
#pragma unroll
      for (int ww = 0; ww < 4; ++ww) {
        const float wt = __builtin_amdgcn_exp2f((ml[ww][0] - M) * C2);
        Ls = fmaf(wt, ml[ww][1], Ls);
        o += wt * *(const f32x4*)&L.mrg[ww][8 + 4 * lane];
      }
      o *= __builtin_amdgcn_rcpf(Ls);
      const unsigned lse = __float_as_uint(fmaf(M, C2, __builtin_amdgcn_logf(Ls))) | 1u;
      const int po = WS_P1A + (((2 * kvh + hh) * NB + b) * NSPLIT + split) * 1024 + (lane & 31) * 32;
      __builtin_amdgcn_raw_buffer_store_b128(u32x4{__float_as_uint(o[0]), lse, __float_as_uint(o[1]), lse}, rs, po, 0, AUX_SC1);
      __builtin_amdgcn_raw_buffer_store_b128(u32x4{__float_as_uint(o[2]), lse, __float_as_uint(o[3]), lse}, rs, po + 16, 0, AUX_SC1);
    }
    STAMP(6);
    {
      const int hh = lane & 31, u = 2 * sw + (lane >> 5);
      const int off = WS_P2 + ((cg * HQ + hh) * 288 + bo * 72 + 9 * (h & 7)) * 4;
      while (lds_peek(sy_addr + 20) < 4u) __builtin_amdgcn_s_sleep(4);
      float y = bf2f(b2[0]), y8 = y;
#pragma unroll
      for (int ww = 0; ww < 4; ++ww) { y += L.red2[ww][u]; y8 += L.red2[ww][8]; }
      float gate = y * __builtin_amdgcn_rcpf(1.0f + __expf(-y)), gate8 = y8 * __builtin_amdgcn_rcpf(1.0f + __expf(-y8));
      float xres = bf2f(x[bo * DM + d0 + u]), xres8 = bf2f(x[bo * DM + d0 + 8]);
      asm volatile("" : "+v"(gate), "+v"(gate8), "+v"(xres), "+v"(xres8));
      unsigned g, g8 = 1u;
      while (lds_peek(sy_addr + 20) < 4u + H2GATE) __builtin_amdgcn_s_sleep(2);
#pragma nounroll
      for (unsigned spins = 0; spins < SPIN_CAP; ++spins) {
        asm volatile("" ::: "memory");
        g = __builtin_amdgcn_raw_buffer_load_b32(rs, off + u * 4, 0, AUX_SC1);
        if (sw == 0) g8 = __builtin_amdgcn_raw_buffer_load_b32(rs, off + 32, 0, AUX_SC1);
        if (__all(g & g8 & 1u)) break;
        __builtin_amdgcn_s_sleep(H2RETRY);
      }
      STAMP(7);
      const float s = x16_sum(row_sum(__uint_as_float(g)));
      if (hh == 0) out[bo * DM + d0 + u] = (unsigned short)(cvtpk(xres + s * gate, 0.f) & 0xffffu);
      if (sw == 0) {
        const float s8 = x16_sum(row_sum(__uint_as_float(g8)));
        if (lane == 0) {
          out[bo * DM + d0 + 8] = (unsigned short)(cvtpk(xres8 + s8 * gate8, 0.f) & 0xffffu);
          if (jb == 0) __builtin_amdgcn_raw_buffer_store_b64(*(const u32x2*)&L.sy[6], rs, WS_E, 0, AUX_SC1);
        }
      }
    }
    STAMP(8);
  }
}

extern "C" void kernel_launch(void* const* d_in, const int* in_sizes, int n_in, void* d_out, int out_size, void* d_ws, size_t ws_size,
                              hipStream_t stream) {
  k_fused<<<NWG, NTHR, 0, stream>>>((const unsigned short*)d_in[0], (const unsigned short*)d_in[1], (const unsigned short*)d_in[2],
                                    (const unsigned short*)d_in[3], (const unsigned short*)d_in[4], (const unsigned short*)d_in[6],
                                    (const unsigned short*)d_in[7], (const unsigned short*)d_in[8], (const unsigned short*)d_in[9],
                                    (unsigned char*)d_ws, (unsigned short*)d_out);
}
```

```cpp
#include <hip/hip_runtime.h>
#include <hip/hip_bf16.h>
#include <stdint.h>
#pragma clang diagnostic ignored "-Winline-asm"

#define GAS __attribute__((address_space(1)))
typedef unsigned u32x4 __attribute__((ext_vector_type(4)));
typedef unsigned u32x2 __attribute__((ext_vector_type(2)));
typedef float f32x4 __attribute__((ext_vector_type(4)));
typedef float f32x2 __attribute__((ext_vector_type(2)));
typedef short bf16x8 __attribute__((ext_vector_type(8)));
typedef __bf16 bf16x2 __attribute__((ext_vector_type(2)));
typedef unsigned long long u64;

constexpr int NB = 4, DM = 576, HQ = 32, HKV = 16, SK = 8192, DH = 128, FF = 4608;
constexpr int NWG = 256, NW = 8, NTHR = 512;
#define DEPTH 4
#define PACE 2
#define NL 3
#define ROT 4
#define QSLEEP 3
#define QRETRY 8
#define H1RETRY 4
#define H2RETRY 4
#define H2GATE 4
#define STAG 0
#define STAG0 8
constexpr int NSPLIT = 4, KEYS_WG = SK / NSPLIT;
constexpr int NT = KEYS_WG / (16 * NW);
constexpr float C2 = 0.088388347648318440f * 1.4426950408889634f;
constexpr unsigned SPIN_CAP = 1u << 16;

constexpr int WS_E = 0;
constexpr int WS_FL = 4096;
constexpr int WS_Q0 = 8192;
constexpr int WS_P1A = WS_Q0 + NB * HKV * 4 * 256 * 8;
constexpr int WS_P2 = WS_P1A + HQ * NB * NSPLIT * 128 * 8;
constexpr int WS_END = WS_P2 + 8 * HQ * NB * 72 * 4;
constexpr int WS_STAMP = 4 << 20;
constexpr int AUX_SC1 = 16;

__device__ __forceinline__ float bf2f(unsigned short v) { return __builtin_bit_cast(float, (unsigned)v << 16); }
__device__ __forceinline__ float bflo(unsigned v) { return __builtin_bit_cast(float, v << 16); }
__device__ __forceinline__ float bfhi(unsigned v) { return __builtin_bit_cast(float, v & 0xffff0000u); }
__device__ __forceinline__ unsigned cvtpk(float lo, float hi) {
  bf16x2 r = {(__bf16)lo, (__bf16)hi};
  return __builtin_bit_cast(unsigned, r);
}
__device__ __forceinline__ float dot2(unsigned k, unsigned q, float acc) {
  return __builtin_amdgcn_fdot2_f32_bf16(__builtin_bit_cast(bf16x2, k), __builtin_bit_cast(bf16x2, q), acc, false);
}
template <int CTRL> __device__ __forceinline__ float dpp(float x) {
  return __builtin_bit_cast(float, __builtin_amdgcn_mov_dpp(__builtin_bit_cast(int, x), CTRL, 0xf, 0xf, true));
}
constexpr int XOR1 = 0xB1, XOR2 = 0x4E, XOR7 = 0x141, ROR8 = 0x128;
__device__ __forceinline__ float x16_sum(float x) {
  auto s = __builtin_amdgcn_permlane16_swap(__float_as_uint(x), __float_as_uint(x), false, false);
  return __uint_as_float(s[0]) + __uint_as_float(s[1]);
}
__device__ __forceinline__ float xrow_max(float x) {
  auto s = __builtin_amdgcn_permlane16_swap(__float_as_uint(x), __float_as_uint(x), false, false);
  x = fmaxf(__uint_as_float(s[0]), __uint_as_float(s[1]));
  auto t = __builtin_amdgcn_permlane32_swap(__float_as_uint(x), __float_as_uint(x), false, false);
  return fmaxf(__uint_as_float(t[0]), __uint_as_float(t[1]));
}
__device__ __forceinline__ float xrow_sum(float x) {
  x = x16_sum(x);
  auto t = __builtin_amdgcn_permlane32_swap(__float_as_uint(x), __float_as_uint(x), false, false);
  return __uint_as_float(t[0]) + __uint_as_float(t[1]);
}
__device__ __forceinline__ float row_sum(float x) {
  x += dpp<XOR1>(x); x += dpp<XOR2>(x); x += dpp<XOR7>(x); x += dpp<ROR8>(x);
  return x;
}
__device__ __forceinline__ float wave_sum(float x) { return xrow_sum(row_sum(x)); }
__device__ __forceinline__ f32x4 mfma16(u32x4 a, u32x4 b, f32x4 c) {
  asm volatile("s_nop 1\n\tv_mfma_f32_16x16x32_bf16 %0, %1, %2, %0\n\ts_nop 7" : "+v"(c) : "v"(a), "v"(b));
  return c;
}
__device__ __forceinline__ u32x4 ldnt(const GAS u32x4* p) { return __builtin_nontemporal_load(p); }
#define SB() do { asm volatile("" ::: "memory"); __builtin_amdgcn_sched_barrier(0); } while (0)
#define ORDER() __builtin_amdgcn_fence(__ATOMIC_ACQUIRE, "wavefront")

#define STAMP(k) do {} while (0)
#define LAS __attribute__((address_space(3)))
#define RING_CLOBBERS "a0", "a1", "a2", "a3", "a4", "a5", "a6", "a7", "a8", "a9", "a10", "a11", "a12", "a13", "a14", "a15", "a16", "a17", "a18", "a19", "a20", "a21", "a22", "a23", "a24", "a25", "a26", "a27", "a28", "a29", "a30", "a31", "a32", "a33", "a34", "a35", "a36", "a37", "a38", "a39", "a40", "a41", "a42", "a43", "a44", "a45", "a46", "a47", "a48", "a49", "a50", "a51", "a52", "a53", "a54", "a55", "a56", "a57", "a58", "a59", "a60", "a61", "a62", "a63", "a64", "a65", "a66", "a67", "a68", "a69", "a70", "a71", "a72", "a73", "a74", "a75", "a76", "a77", "a78", "a79", "a80", "a81", "a82", "a83", "a84", "a85", "a86", "a87", "a88", "a89", "a90", "a91", "a92", "a93", "a94", "a95", "a96", "a97", "a98", "a99", "a100", "a101", "a102", "a103", "a104", "a105", "a106", "a107", "a108", "a109", "a110", "a111", "a112", "a113", "a114", "a115", "a116", "a117", "a118", "a119", "a120", "a121", "a122", "a123", "a124", "a125", "a126", "a127"

struct Lds {
  u32x4 kv[4][NL > 0 ? NL : 1][8][64];
  unsigned char wt[4][5120];
  unsigned short q[256];
  unsigned short o[NB][DH];
  float st1[NB][2];
  float red2[4][12];
  float mrg[4][264];
  __attribute__((aligned(16))) unsigned sy[8];
};
constexpr int NTILES = KEYS_WG / 16;
static_assert(DEPTH <= 4, "the ring lives in a[0:127]");
constexpr int VMW = 4 + 8 * (DEPTH - 1);

template <int SL> __device__ __forceinline__ void ring_fill(u32x4 rk, u32x4 rv, int koff, int voff, int soff) {
  asm volatile(
      "s_nop 4\n\t"
      "buffer_load_dwordx4 a[%c0+0:%c0+3], %1, %3, %5 offen sc1 nt\n\t"
      "buffer_load_dwordx4 a[%c0+4:%c0+7], %1, %3, %5 offen offset:64 sc1 nt\n\t"
      "buffer_load_dwordx4 a[%c0+8:%c0+11], %1, %3, %5 offen offset:128 sc1 nt\n\t"
      "buffer_load_dwordx4 a[%c0+12:%c0+15], %1, %3, %5 offen offset:192 sc1 nt\n\t"
      "buffer_load_dwordx4 a[%c0+16:%c0+19], %2, %4, %5 offen sc1 nt\n\t"
      "buffer_load_dwordx4 a[%c0+20:%c0+23], %2, %4, %5 offen offset:256 sc1 nt\n\t"
      "buffer_load_dwordx4 a[%c0+24:%c0+27], %2, %4, %5 offen offset:512 sc1 nt\n\t"
      "buffer_load_dwordx4 a[%c0+28:%c0+31], %2, %4, %5 offen offset:768 sc1 nt"
      :: "i"(32 * SL), "v"(koff), "v"(voff), "s"(rk), "s"(rv), "s"(soff) : "memory", RING_CLOBBERS);
}
template <int SL> __device__ __forceinline__ void step_qk(f32x4& S, int& idx, int& soff, const u32x4 (&qf)[4], u32x4 rk, int koff,
                                                          unsigned next_addr, unsigned one, int rot) {
  unsigned g;
  asm volatile(
      "s_mov_b64 exec, 1\n\t"
      "ds_add_rtn_u32 %3, %11, %12\n\t"
      "s_mov_b64 exec, -1\n\t"
      "s_waitcnt vmcnt(%c13)\n\t"
      "v_mfma_f32_16x16x32_bf16 %0, a[%c4+0:%c4+3], %5, 0\n\t"
      "v_mfma_f32_16x16x32_bf16 %0, a[%c4+4:%c4+7], %6, %0\n\t"
      "v_mfma_f32_16x16x32_bf16 %0, a[%c4+8:%c4+11], %7, %0\n\t"
      "v_mfma_f32_16x16x32_bf16 %0, a[%c4+12:%c4+15], %8, %0\n\t"
      "s_waitcnt lgkmcnt(0)\n\t"
      "v_readfirstlane_b32 %1, %3\n\t"
      "s_nop 0\n\t"
      "s_add_u32 %2, %1, %14\n\t"
      "s_and_b32 %2, %2, 127\n\t"
      "s_lshl_b32 %2, %2, 12\n\t"
      "s_cmp_lt_u32 %1, 128\n\t"
      "s_cselect_b32 %2, %2, 0x40000000\n\t"
      "buffer_load_dwordx4 a[%c4+0:%c4+3], %10, %9, %2 offen sc1 nt\n\t"
      "buffer_load_dwordx4 a[%c4+4:%c4+7], %10, %9, %2 offen offset:64 sc1 nt\n\t"
      "buffer_load_dwordx4 a[%c4+8:%c4+11], %10, %9, %2 offen offset:128 sc1 nt\n\t"
      "buffer_load_dwordx4 a[%c4+12:%c4+15], %10, %9, %2 offen offset:192 sc1 nt\n\t"
      "s_nop 1"
      : "=&v"(S), "=&s"(idx), "=&s"(soff), "=&v"(g)
      : "i"(32 * SL), "v"(qf[0]), "v"(qf[1]), "v"(qf[2]), "v"(qf[3]), "s"(rk), "v"(koff), "v"(next_addr), "v"(one), "i"(VMW), "s"(rot)
      : "memory", "scc", RING_CLOBBERS);
}
template <int SL> __device__ __forceinline__ void step_pv(f32x4& acc, const u32x4 (&bb)[4], u32x4 rv, int voff, int soff) {
  asm volatile(
      "s_waitcnt vmcnt(%c9)\n\t"
      "s_nop 0\n\t"
      "v_mfma_f32_16x16x32_bf16 %0, a[%c1+16:%c1+19], %2, %0\n\t"
      "v_mfma_f32_16x16x32_bf16 %0, a[%c1+20:%c1+23], %3, %0\n\t"
      "v_mfma_f32_16x16x32_bf16 %0, a[%c1+24:%c1+27], %4, %0\n\t"
      "v_mfma_f32_16x16x32_bf16 %0, a[%c1+28:%c1+31], %5, %0\n\t"
      "buffer_load_dwordx4 a[%c1+16:%c1+19], %7, %6, %8 offen sc1 nt\n\t"
      "buffer_load_dwordx4 a[%c1+20:%c1+23], %7, %6, %8 offen offset:256 sc1 nt\n\t"
      "buffer_load_dwordx4 a[%c1+24:%c1+27], %7, %6, %8 offen offset:512 sc1 nt\n\t"
      "buffer_load_dwordx4 a[%c1+28:%c1+31], %7, %6, %8 offen offset:768 sc1 nt\n\t"
      "s_nop 4"
      : "+v"(acc)
      : "i"(32 * SL), "v"(bb[0]), "v"(bb[1]), "v"(bb[2]), "v"(bb[3]), "s"(rv), "v"(voff), "s"(soff), "i"(VMW)
      : "memory", RING_CLOBBERS);
}
__device__ __forceinline__ void lds_fill(u32x4 rk, u32x4 rv, int koff, int voff, int soff, unsigned dst) {
  unsigned t;
  asm volatile(
      "s_mov_b32 m0, %6\n\ts_add_u32 %0, %6, 960\n\t"
      "buffer_load_dwordx4 %1, %3, %5 offen sc1 nt lds\n\t"
      "s_mov_b32 m0, %0\n\ts_add_u32 %0, %0, 960\n\t"
      "buffer_load_dwordx4 %1, %3, %5 offen offset:64 sc1 nt lds\n\t"
      "s_mov_b32 m0, %0\n\ts_add_u32 %0, %0, 960\n\t"
      "buffer_load_dwordx4 %1, %3, %5 offen offset:128 sc1 nt lds\n\t"
      "s_mov_b32 m0, %0\n\ts_add_u32 %0, %6, 4096\n\t"
      "buffer_load_dwordx4 %1, %3, %5 offen offset:192 sc1 nt lds\n\t"
      "s_mov_b32 m0, %0\n\ts_add_u32 %0, %0, 768\n\t"
      "buffer_load_dwordx4 %2, %4, %5 offen sc1 nt lds\n\t"
      "s_mov_b32 m0, %0\n\ts_add_u32 %0, %0, 768\n\t"
      "buffer_load_dwordx4 %2, %4, %5 offen offset:256 sc1 nt lds\n\t"
      "s_mov_b32 m0, %0\n\ts_add_u32 %0, %0, 768\n\t"
      "buffer_load_dwordx4 %2, %4, %5 offen offset:512 sc1 nt lds\n\t"
      "s_mov_b32 m0, %0\n\ts_nop 0\n\t"
      "buffer_load_dwordx4 %2, %4, %5 offen offset:768 sc1 nt lds"
      : "=&s"(t) : "v"(koff), "v"(voff), "s"(rk), "s"(rv), "s"(soff), "s"(dst) : "memory", "scc", "m0");
}
__device__ __forceinline__ void dma16(const void* g, unsigned dst) {
  asm volatile("s_mov_b32 m0, %1\n\ts_nop 0\n\tglobal_load_lds_dwordx4 %0, off nt" :: "v"(g), "s"(dst) : "memory", "m0");
}
__device__ __forceinline__ void mfma4(f32x4& d, const u32x4 (&a)[4], const u32x4 (&b)[4]) {
  asm volatile("s_nop 1\n\t"
               "v_mfma_f32_16x16x32_bf16 %0, %1, %5, %0\n\tv_mfma_f32_16x16x32_bf16 %0, %2, %6, %0\n\t"
               "v_mfma_f32_16x16x32_bf16 %0, %3, %7, %0\n\tv_mfma_f32_16x16x32_bf16 %0, %4, %8, %0\n\ts_nop 7"
               : "+v"(d) : "v"(a[0]), "v"(a[1]), "v"(a[2]), "v"(a[3]), "v"(b[0]), "v"(b[1]), "v"(b[2]), "v"(b[3]));
}
typedef short s16x4 __attribute__((ext_vector_type(4)));
__device__ __forceinline__ u32x4 tr_frag(const unsigned char* p, int step) {
  const u32x2 lo = __builtin_bit_cast(u32x2, __builtin_amdgcn_ds_read_tr16_b64_v4i16((LAS s16x4*)p));
  const u32x2 hi = __builtin_bit_cast(u32x2, __builtin_amdgcn_ds_read_tr16_b64_v4i16((LAS s16x4*)(p + step)));
  return u32x4{lo[0], lo[1], hi[0], hi[1]};
}
__device__ __forceinline__ u32x4 make_rsrc(const void* p, unsigned bytes) {
  const u64 a = (u64)p;
  u32x4 r = {(unsigned)__builtin_amdgcn_readfirstlane((unsigned)a), (unsigned)__builtin_amdgcn_readfirstlane((unsigned)(a >> 32) & 0xffffu), bytes, 0x00020000u};
  return r;
}
#define NTS ""
#define LD16(dst, p, OFF) asm volatile("global_load_dwordx4 %0, %1, off offset:%c2" NTS : "=v"(dst) : "v"(p), "i"(OFF) : "memory")
#define LDU16(dst, p, OFF) asm volatile("global_load_ushort %0, %1, off offset:%c2" NTS : "=v"(dst) : "v"(p), "i"(OFF) : "memory")
#define LD4(dst, p, OFF) asm volatile("global_load_dword %0, %1, off offset:%c2" NTS : "=v"(dst) : "v"(p), "i"(OFF) : "memory")
#define TIE8(a) asm volatile("" : "+v"(a[0]), "+v"(a[1]), "+v"(a[2]), "+v"(a[3]), "+v"(a[4]), "+v"(a[5]), "+v"(a[6]), "+v"(a[7]))

__device__ __forceinline__ unsigned lds_peek(unsigned addr) {
  unsigned f;
  asm volatile("ds_read_b32 %0, %1\n\ts_waitcnt lgkmcnt(0)" : "=v"(f) : "v"(addr) : "memory");
  return __builtin_amdgcn_readfirstlane(f);
}
__device__ __forceinline__ void lds_arrive(unsigned addr, unsigned one) {
  asm volatile("s_mov_b64 exec, 1\n\tds_add_u32 %0, %1\n\ts_mov_b64 exec, -1" :: "v"(addr), "v"(one) : "memory");
}

__global__ void __launch_bounds__(NTHR, 2)
k_fused(const unsigned short* __restrict__ x, const unsigned short* __restrict__ kc, const unsigned short* __restrict__ vc,
        const unsigned short* __restrict__ wq, const unsigned short* __restrict__ wo, const unsigned short* __restrict__ w1,
        const unsigned short* __restrict__ b1, const unsigned short* __restrict__ w2, const unsigned short* __restrict__ b2,
        unsigned char* ws, unsigned short* __restrict__ out) {
  __shared__ __attribute__((aligned(16))) Lds L;
  const int tid = threadIdx.x, lane = tid & 63, w = __builtin_amdgcn_readfirstlane(tid >> 6);
  STAMP(0);
  if (tid == 0) {
    *(u32x4*)&L.sy[0] = u32x4{4u, 0u, 0u, 0u};
    *(u32x4*)&L.sy[4] = u32x4{0u, 0u, 0u, 0u};
  }
  asm volatile("s_waitcnt lgkmcnt(0)\n\ts_barrier" :: "s"(x), "s"(kc), "s"(vc), "s"(wq), "s"(ws) : "memory");
  const int jb = blockIdx.x;
  const int kvh = 2 * (jb & 7) + ((jb >> 3) & 1), gi = jb >> 4;
  const int b = gi >> 2, split = gi & 3;
  const int h = 2 * kvh + (gi >> 3), cg = gi & 7;
  const int bo = h >> 3, d0 = 72 * cg + 9 * (h & 7);
  const int c = lane & 15, hl = c >> 3, g4 = lane >> 4;
  const __amdgpu_buffer_rsrc_t rs = __builtin_amdgcn_make_buffer_rsrc((void*)ws, 0, WS_END, 0x00020000);
  const unsigned sy_addr = (unsigned)(size_t)(LAS unsigned*)&L.sy[0];
  const unsigned one = 1u;
  const bool xact = lane < 36;
#define UNPACK16(f, v0, v1) do { _Pragma("unroll") for (int k = 0; k < 4; ++k) { \
    f[2 * k] = bflo(v0[k]); f[2 * k + 1] = bfhi(v0[k]); f[8 + 2 * k] = bflo(v1[k]); f[8 + 2 * k + 1] = bfhi(v1[k]); } } while (0)
#define LN_STATS(f, mean, rstd) do { float s_ = 0.f; _Pragma("unroll") for (int k = 0; k < 16; ++k) s_ += f[k]; \
    mean = wave_sum(xact ? s_ : 0.f) * (1.f / DM); float q_ = 0.f; \
    _Pragma("unroll") for (int k = 0; k < 16; ++k) { f[k] -= mean; q_ = fmaf(f[k], f[k], q_); } \
    rstd = __builtin_amdgcn_rsqf(wave_sum(xact ? q_ : 0.f) * (1.f / DM) + 1e-5f); } while (0)

  if (__builtin_expect(w < 4, 1)) {
    __builtin_amdgcn_s_setprio(3);
    const unsigned wt_addr = (unsigned)(size_t)(LAS unsigned char*)&L.wt[w][0];
    u32x2 e1;
    {
      u32x4 xa[5], xv0, xv1;
      u32x2 ev;
      const int r0 = 144 * (gi >> 2);
      {
        const unsigned short* xr = x + w * DM + 8 * (xact ? lane : 0);
        LD16(xv0, xr, 0); LD16(xv1, xr, 576);
        const unsigned short* wp = wq + (size_t)(r0 + 36 * w + (lane >> 3)) * 4096 + 256 * kvh + 64 * (gi & 3) + 8 * (lane & 7);
        const unsigned short* wp2 = wq + (size_t)(r0 + 36 * w + 32 + ((lane >> 3) & 3)) * 4096 + 256 * kvh + 64 * (gi & 3) + 8 * (lane & 7);
        dma16(wp, wt_addr); dma16(wp + 8 * 4096, wt_addr + 1024); dma16(wp + 16 * 4096, wt_addr + 2048);
        dma16(wp + 24 * 4096, wt_addr + 3072); dma16(wp2, wt_addr + 4096);
        const unsigned short* xp = x + (c >> 2) * DM + r0 + 8 * g4;
        const unsigned short* xp2 = x + (c >> 2) * DM + r0 + 36 * g4 + 32;
        LD16(xa[0], xp, 0); LD16(xa[1], xp, 72); LD16(xa[2], xp, 144); LD16(xa[3], xp, 216);
        asm volatile("global_load_dwordx2 %0, %1, off" : "=v"(*(u32x2*)&xa[4]) : "v"(xp2) : "memory");
        asm volatile("global_load_dwordx2 %0, %1, off sc1" : "=v"(ev) : "v"(ws + WS_E) : "memory");
      }
      STAMP(1);
      asm volatile("s_waitcnt vmcnt(11)" : "+v"(xv0), "+v"(xv1) :: "memory");
      {
        float f[16], mean, rstd;
        UNPACK16(f, xv0, xv1);
        LN_STATS(f, mean, rstd);
        if (lane == 0) *(f32x2*)&L.st1[w][0] = f32x2{mean, rstd};
      }
      asm volatile("s_waitcnt vmcnt(0)" : "+v"(xa[0]), "+v"(xa[1]), "+v"(xa[2]), "+v"(xa[3]), "+v"(*(u32x2*)&xa[4]), "+v"(ev) :: "memory");
      lds_arrive(sy_addr + 4, one);
      STAMP(2);
      u32x4 a[4], a4;
#pragma unroll
      for (int s = 0; s < 4; ++s)
#pragma unroll
        for (int k = 0; k < 4; ++k) a[s][k] = (c & 3) == 0 ? xa[s][k] : ((c & 3) == 1 ? 0x3F803F80u : 0u);
#pragma unroll
      for (int k = 0; k < 4; ++k) a4[k] = k >= 2 ? 0u : (c & 3) == 0 ? xa[4][k] : ((c & 3) == 1 ? 0x3F803F80u : 0u);
      const u64 e1w = (((u64)ev[1] << 32) | ev[0]) + 1ull;
      e1 = u32x2{(unsigned)e1w, (unsigned)(e1w >> 32)};
      const unsigned tq_lo = e1[0] & 0xffu, tq_hi = (unsigned)(e1w >> 8);
      const int po = WS_Q0 + (((g4 * HKV + kvh) * 4 + (gi >> 2)) * 256 + 64 * (gi & 3) + 16 * w + c) * 8;
      const int qoff = WS_Q0 + ((b * HKV + kvh) * 1024 + 64 * w + lane) * 8;
      asm volatile("" : "+v"(a[0]), "+v"(a[1]), "+v"(a[2]), "+v"(a[3]), "+v"(a4) : "v"(po), "v"(qoff), "v"(tq_lo), "v"(tq_hi));
      while (lds_peek(sy_addr + 4) != 4u) {}
      f32x4 acc = {0.f, 0.f, 0.f, 0.f};
      {
        const unsigned char* tp = &L.wt[0][(8 * g4 + (c >> 2)) * 128 + 32 * w + (c & 3) * 8];
        const unsigned char* tp2 = &L.wt[g4][(32 + (c >> 2)) * 128 + 32 * w + (c & 3) * 8];
        u32x4 bb[4];
#pragma unroll
        for (int s = 0; s < 4; ++s) bb[s] = tr_frag(tp + 5120 * s, 512);
        const u32x2 lo = __builtin_bit_cast(u32x2, __builtin_amdgcn_ds_read_tr16_b64_v4i16((LAS s16x4*)tp2));
        mfma4(acc, a, bb);
        acc = mfma16(a4, u32x4{lo[0], lo[1], 0u, 0u}, acc);
      }
      const f32x2 st = *(const f32x2*)&L.st1[g4][0];
      const float qv = (acc[0] - st[0] * acc[1]) * st[1];
      __builtin_amdgcn_raw_buffer_store_b64(u32x2{((__float_as_uint(qv) + 0x80u) & 0xffffff00u) | tq_lo, tq_hi}, rs, po, 0, AUX_SC1);
      STAMP(3);
      u32x2 g[4];
      for (int i = 0; i < QSLEEP; ++i) __builtin_amdgcn_s_sleep(16);
#pragma nounroll
      for (unsigned spins = 0; spins < SPIN_CAP; ++spins) {
        asm volatile("" ::: "memory");
#pragma unroll
        for (int k = 0; k < 4; ++k) g[k] = __builtin_amdgcn_raw_buffer_load_b64(rs, qoff + 2048 * k, 0, AUX_SC1);
        unsigned bad = 0;
#pragma unroll
        for (int k = 0; k < 4; ++k) bad |= ((g[k][0] ^ tq_lo) & 0xffu) | (g[k][1] ^ tq_hi);
        if (__all(bad == 0)) break;
        __builtin_amdgcn_s_sleep(QRETRY);
      }
      float qs = 0.f;
#pragma unroll
      for (int k = 0; k < 4; ++k) qs += __uint_as_float(g[k][0] & 0xffffff00u);
      L.q[64 * w + lane] = (unsigned short)(cvtpk(qs, 0.f) & 0xffffu);
      lds_arrive(sy_addr + 8, one);
      STAMP(4);
    }
    __builtin_amdgcn_s_setprio(0);
    if (w == 0) *(u32x2*)&L.sy[6] = e1;
    for (int i = 0; i < STAG0 + STAG * (jb >> 3); ++i) __builtin_amdgcn_s_sleep(16);
    while (lds_peek(sy_addr + 8) != 4u) __builtin_amdgcn_s_sleep(4);
    __builtin_amdgcn_s_setprio(3);
    {
      const u32x4 z = {0u, 0u, 0u, 0u};
      if (w < 2) {
        __builtin_amdgcn_raw_buffer_store_b128(z, rs, WS_P1A + (((2 * kvh + w) * NB + b) * NSPLIT + split) * 1024 + lane * 16, 0, AUX_SC1);
      } else {
        __builtin_amdgcn_raw_buffer_store_b128(z, rs, WS_P2 + (cg * HQ + h) * 1152 + (w - 2) * 128 + lane * 16, 0, AUX_SC1);
      }
    }
    {
      u32x4 xv0, xv1, m1v[2], mbv[2], m2v[2];
      unsigned m1s, mbs, m2s, xs;
      {
        const unsigned short* xr = x + bo * DM + 8 * (xact ? lane : 0);
        LD16(xv0, xr, 0); LD16(xv1, xr, 576);
        LDU16(xs, x + bo * DM + d0 + (lane < 9 ? lane : 0), 0);
        LD16(m1v[0], w1 + 8 * tid, 0); LD16(mbv[0], b1 + 8 * tid, 0); LD16(m2v[0], w2 + 8 * tid, 0);
        LD16(m1v[1], w1 + 2048 + 8 * tid, 0); LD16(mbv[1], b1 + 2048 + 8 * tid, 0); LD16(m2v[1], w2 + 2048 + 8 * tid, 0);
        LD4(m1s, w1 + 4096 + 2 * tid, 0); LD4(mbs, b1 + 4096 + 2 * tid, 0); LD4(m2s, w2 + 4096 + 2 * tid, 0);
      }
      {
        const unsigned short* wop = wo + (size_t)(128 * h + 32 * w) * DM + 72 * cg;
#pragma unroll
        for (int t = 0; t < 5; ++t) {
          const int i = t < 4 ? 64 * t + lane : 256 + (lane & 31);
          const int row = (i * 7282) >> 16;
          dma16(wop + row * DM + 8 * (i - 9 * row), wt_addr + 1024 * t);
        }
      }
      __builtin_amdgcn_s_setprio(0);
      asm volatile("s_waitcnt vmcnt(5)" : "+v"(xv0), "+v"(xv1), "+v"(xs), "+v"(m1v[0]), "+v"(mbv[0]), "+v"(m2v[0]), "+v"(m1v[1]), "+v"(mbv[1]), "+v"(m2v[1]) :: "memory");
      asm volatile("" : "+v"(m1s), "+v"(mbs), "+v"(m2s));
      STAMP(5);
      float hv[9];
      {
        float f[16], mean, rstd, mean2, rstd2;
        UNPACK16(f, xv0, xv1);
        LN_STATS(f, mean, rstd);
        u32x4 o0, o1;
#pragma unroll
        for (int k = 0; k < 4; ++k) { o0[k] = cvtpk(f[2 * k] * rstd, f[2 * k + 1] * rstd); o1[k] = cvtpk(f[8 + 2 * k] * rstd, f[8 + 2 * k + 1] * rstd); }
        UNPACK16(f, o0, o1);
        LN_STATS(f, mean2, rstd2);
        const float xn = bflo(cvtpk((bflo(xs) - mean) * rstd, 0.f));
        const unsigned hb = cvtpk(0.f, (xn - mean2) * rstd2);
#pragma unroll
        for (int u = 0; u < 9; ++u) hv[u] = __builtin_bit_cast(float, __builtin_amdgcn_readlane(hb, u));
      }
      constexpr float G0 = -2.0f * 0.7978845608028654f * 1.4426950408889634f, G1 = G0 * 0.044715f;
      f32x2 ya[9];
#pragma unroll
      for (int u = 0; u < 9; ++u) ya[u] = f32x2{0.f, 0.f};
#define NMS 9
#pragma unroll
      for (int ms = 0; ms < NMS; ++ms) {
        const unsigned r1 = ms < 8 ? m1v[ms >> 2][ms & 3] : m1s, rb = ms < 8 ? mbv[ms >> 2][ms & 3] : mbs, r2 = ms < 8 ? m2v[ms >> 2][ms & 3] : m2s;
        const f32x2 A1 = {bflo(r1), bfhi(r1)}, C1 = {bflo(rb), bfhi(rb)}, A2 = {bflo(r2), bfhi(r2)};
#pragma unroll
        for (int u = 0; u < 9; ++u) {
          const f32x2 uu = A1 * hv[u] + C1;
          const f32x2 z = uu * (uu * uu * G1 + G0);
          const f32x2 e = f32x2{__builtin_amdgcn_exp2f(z[0]), __builtin_amdgcn_exp2f(z[1])} + 1.0f;
          const f32x2 gl = uu * f32x2{__builtin_amdgcn_rcpf(e[0]), __builtin_amdgcn_rcpf(e[1])};
          ya[u] = gl * A2 + ya[u];
        }
      }
#pragma unroll
      for (int u = 0; u < 9; ++u) { const float s = wave_sum(ya[u][0] + ya[u][1]); if (lane == 0) L.red2[w][u] = s; }
      asm volatile("s_waitcnt vmcnt(0)" ::: "memory");
      lds_arrive(sy_addr + 12, one);
      STAMP(6);
    }
    while (lds_peek(sy_addr + 12) != 4u) __builtin_amdgcn_s_sleep(8);
    u32x4 wf[5][4];
#pragma unroll
    for (int nt = 0; nt < 5; ++nt)
#pragma unroll
      for (int ks = 0; ks < 4; ++ks) wf[nt][ks] = tr_frag(&L.wt[ks][(8 * g4 + (c >> 2)) * 144 + (c & 3) * 8 + 32 * nt], 576);
    __builtin_amdgcn_s_setprio(3);
    if (tid == 0) __builtin_amdgcn_raw_buffer_store_b64(e1, rs, WS_FL + jb * 8, 0, AUX_SC1);
    while (lds_peek(sy_addr) < (unsigned)(NTILES - 16)) __builtin_amdgcn_s_sleep(32);
    for (unsigned spins = 0; spins < SPIN_CAP; ++spins) {
      asm volatile("" ::: "memory");
      const u32x2 f = __builtin_amdgcn_raw_buffer_load_b64(rs, WS_FL + tid * 8, 0, AUX_SC1);
      if (__all(f[0] == e1[0] && f[1] == e1[1])) break;
      __builtin_amdgcn_s_sleep(8);
    }
    lds_arrive(sy_addr + 20, one);
    while (lds_peek(sy_addr + 20) < 4u) __builtin_amdgcn_s_sleep(2);
    while (lds_peek(sy_addr + 16) != 4u) __builtin_amdgcn_s_sleep(4);
    STAMP(7);
    {
      const int off = WS_P1A + ((h * NB + w) * NSPLIT) * 1024 + lane * 16;
      u32x4 g[NSPLIT];
      unsigned pend = 15u;
      for (unsigned spins = 0; spins < SPIN_CAP; ++spins) {
        asm volatile("" ::: "memory");
#pragma unroll
        for (int s = 0; s < NSPLIT; ++s)
          if (pend & (1u << s)) g[s] = __builtin_amdgcn_raw_buffer_load_b128(rs, off + s * 1024, 0, AUX_SC1);
#pragma unroll
        for (int s = 0; s < NSPLIT; ++s)
          if ((pend & (1u << s)) && __all(g[s][1] & g[s][3] & 1u)) pend &= ~(1u << s);
        if (!pend) break;
        __builtin_amdgcn_s_sleep(H1RETRY);
      }
      STAMP(8);
      float M = -INFINITY;
#pragma unroll
      for (int s = 0; s < NSPLIT; ++s) M = fmaxf(M, __uint_as_float(g[s][1]));
      float Ls = 0.f, o0 = 0.f, o1 = 0.f;
#pragma unroll
      for (int s = 0; s < NSPLIT; ++s) {
        const float wt = __builtin_amdgcn_exp2f(__uint_as_float(g[s][1]) - M);
        Ls += wt;
        o0 = fmaf(wt, __uint_as_float(g[s][0]), o0);
        o1 = fmaf(wt, __uint_as_float(g[s][2]), o1);
      }
      const float r = __builtin_amdgcn_rcpf(Ls);
      *(unsigned*)&L.o[w][2 * lane] = cvtpk(o0 * r, o1 * r);
    }
    {
      f32x4 a2[5];
#pragma unroll
      for (int nt = 0; nt < 5; ++nt) a2[nt] = f32x4{0.f, 0.f, 0.f, 0.f};
      u32x4 a[4];
#pragma unroll
      for (int ks = 0; ks < 4; ++ks) a[ks] = *(const u32x4*)&L.o[w][32 * ks + 8 * g4];
      asm volatile("s_nop 1\n\t"
#define M5(ks, a) "v_mfma_f32_16x16x32_bf16 %0, " a ", %" #ks ", %0\n\t"
                   "v_mfma_f32_16x16x32_bf16 %0, %5, %9, %0\n\tv_mfma_f32_16x16x32_bf16 %1, %5, %13, %1\n\tv_mfma_f32_16x16x32_bf16 %2, %5, %17, %2\n\t"
                   "v_mfma_f32_16x16x32_bf16 %3, %5, %21, %3\n\tv_mfma_f32_16x16x32_bf16 %4, %5, %25, %4\n\t"
                   "v_mfma_f32_16x16x32_bf16 %0, %6, %10, %0\n\tv_mfma_f32_16x16x32_bf16 %1, %6, %14, %1\n\tv_mfma_f32_16x16x32_bf16 %2, %6, %18, %2\n\t"
                   "v_mfma_f32_16x16x32_bf16 %3, %6, %22, %3\n\tv_mfma_f32_16x16x32_bf16 %4, %6, %26, %4\n\t"
                   "v_mfma_f32_16x16x32_bf16 %0, %7, %11, %0\n\tv_mfma_f32_16x16x32_bf16 %1, %7, %15, %1\n\tv_mfma_f32_16x16x32_bf16 %2, %7, %19, %2\n\t"
                   "v_mfma_f32_16x16x32_bf16 %3, %7, %23, %3\n\tv_mfma_f32_16x16x32_bf16 %4, %7, %27, %4\n\t"
                   "v_mfma_f32_16x16x32_bf16 %0, %8, %12, %0\n\tv_mfma_f32_16x16x32_bf16 %1, %8, %16, %1\n\tv_mfma_f32_16x16x32_bf16 %2, %8, %20, %2\n\t"
                   "v_mfma_f32_16x16x32_bf16 %3, %8, %24, %3\n\tv_mfma_f32_16x16x32_bf16 %4, %8, %28, %4\n\t"
                   "s_nop 7"
                   : "+v"(a2[0]), "+v"(a2[1]), "+v"(a2[2]), "+v"(a2[3]), "+v"(a2[4])
                   : "v"(a[0]), "v"(a[1]), "v"(a[2]), "v"(a[3]),
                     "v"(wf[0][0]), "v"(wf[0][1]), "v"(wf[0][2]), "v"(wf[0][3]), "v"(wf[1][0]), "v"(wf[1][1]), "v"(wf[1][2]), "v"(wf[1][3]),
                     "v"(wf[2][0]), "v"(wf[2][1]), "v"(wf[2][2]), "v"(wf[2][3]), "v"(wf[3][0]), "v"(wf[3][1]), "v"(wf[3][2]), "v"(wf[3][3]),
                     "v"(wf[4][0]), "v"(wf[4][1]), "v"(wf[4][2]), "v"(wf[4][3]));
      const float v0 = g4 == 0 ? a2[0][0] : g4 == 1 ? a2[1][0] : g4 == 2 ? a2[2][0] : a2[3][0];
      const int po = WS_P2 + ((cg * HQ + h) * 288 + w * 72 + lane) * 4;
      __builtin_amdgcn_raw_buffer_store_b32(__float_as_uint(v0) | 1u, rs, po, 0, AUX_SC1);
      if (lane < 8) __builtin_amdgcn_raw_buffer_store_b32(__float_as_uint(a2[4][0]) | 1u, rs, po + 256, 0, AUX_SC1);
      lds_arrive(sy_addr + 20, one);
    }
    STAMP(9);
  } else {
    const int sw = w - 4;
    __builtin_amdgcn_s_setprio(1);
#define REC() do {} while (0)
    const int rot = ROT * (jb >> 3);
#define TOFF(t) ((((t) + rot) & 127) * 4096)
    const size_t kvbase = ((size_t)(b * HKV + kvh) * SK + (size_t)split * KEYS_WG) * DH;
    const u32x4 rk = make_rsrc(kc + kvbase, KEYS_WG * DH * 2), rv = make_rsrc(vc + kvbase, KEYS_WG * DH * 2);
    const int koff = c * 256 + g4 * 16, voff = g4 * 1024 + c * 16;
    const unsigned kv_addr = (unsigned)(size_t)(LAS u32x4*)&L.kv[sw][0][0][0];
    asm volatile("s_nop 4");
    lds_fill(rk, rv, koff, voff, TOFF(sw), kv_addr);
    STAMP(1);
    unsigned msk[4];
#pragma unroll
    for (int k = 0; k < 4; ++k) msk[k] = ((c & 7) >> 1) == k ? ((c & 1) ? 0xffff0000u : 0x0000ffffu) : 0u;
    f32x4 acc = {0.f, 0.f, 0.f, 0.f};
    float m = -INFINITY, mc = 0.f, l = 0.f;
    int tix[DEPTH];
#pragma unroll
    for (int d = 0; d < DEPTH; ++d) tix[d] = 0;
    u32x4 qf[4];
#pragma unroll
    for (int i = 0; i < 4; ++i) qf[i] = u32x4{0u, 0u, 0u, 0u};

#define SOFTMAX(S, bb) do { \
    float mt = fmaxf(fmaxf(S[0], S[1]), fmaxf(S[2], S[3])); \
    if (__any(mt > m)) { \
      mt = xrow_max(mt); \
      const float mn = fmaxf(m, mt); \
      const float alpha = __builtin_amdgcn_exp2f((m - mn) * C2); \
      m = mn; mc = -mn * C2; acc *= alpha; l *= alpha; \
    } \
    _Pragma("unroll") for (int t = 0; t < 4; ++t) { \
      const float p = __builtin_amdgcn_exp2f(fmaf(S[t], C2, mc)); \
      l += p; \
      const unsigned pp = cvtpk(p, p); \
      bb[t] = u32x4{pp & msk[0], pp & msk[1], pp & msk[2], pp & msk[3]}; } } while (0)
#define TILE(SL) do { \
    f32x4 S; int nx_, so_; u32x4 bb[4]; \
    step_qk<SL>(S, nx_, so_, qf, rk, koff, sy_addr, one, rot); \
    SOFTMAX(S, bb); \
    step_pv<SL>(acc, bb, rv, voff, so_); REC(); \
    tix[SL] = nx_; } while (0)
    int nl = 1, phase = 0;
    for (;;) {
      if (tix[0] >= NTILES) break;
      TILE(0);
      if (tix[1] >= NTILES) break;
      TILE(1);
      if (tix[2] >= NTILES) break;
      TILE(2);
      if (tix[3] >= NTILES) break;
      TILE(3);
      phase = __builtin_amdgcn_readfirstlane(phase);
      asm volatile("" : "+s"(phase));
      if (phase < 2) {
        if (phase == 0) {
          STAMP(2);
          for (; nl < NL; ++nl) {
            asm volatile("s_waitcnt vmcnt(%c0)" :: "i"(8 * (PACE - 1)) : "memory");
            if (lds_peek(sy_addr + 8) == 4u) break;
            unsigned g;
            asm volatile("s_mov_b64 exec, 1\n\tds_add_rtn_u32 %0, %1, %2\n\ts_mov_b64 exec, -1\n\ts_waitcnt lgkmcnt(0)"
                         : "=&v"(g) : "v"(sy_addr), "v"(one) : "memory");
            const int so = TOFF(__builtin_amdgcn_readfirstlane(g));
            const unsigned dst = __builtin_amdgcn_readfirstlane(kv_addr + 8192 * nl);
            asm volatile("s_nop 4" :: "s"(so), "s"(dst));
            lds_fill(rk, rv, koff, voff, so, dst);
          }
          STAMP(3);
          while (lds_peek(sy_addr + 8) != 4u) __builtin_amdgcn_s_sleep(1);
          STAMP(4);
#pragma unroll
          for (int i = 0; i < 4; ++i) qf[i] = *(const u32x4*)&L.q[hl * 128 + 32 * i + 8 * g4];
          acc = f32x4{0.f, 0.f, 0.f, 0.f};
          m = -INFINITY; mc = 0.f; l = 0.f;
        } else {
          asm volatile("s_waitcnt vmcnt(%c0)" :: "i"(8 * DEPTH) : "memory");
          for (; nl > 0; --nl) {
            u32x4 kf[4], vf[4], bb[4];
#pragma unroll
            for (int i = 0; i < 4; ++i) { kf[i] = L.kv[sw][nl - 1][i][lane]; vf[i] = L.kv[sw][nl - 1][4 + i][lane]; }
            f32x4 S = {0.f, 0.f, 0.f, 0.f};
            mfma4(S, kf, qf);
            SOFTMAX(S, bb);
            mfma4(acc, vf, bb); REC();
          }
        }
        ++phase;
      }
    }
    STAMP(5);
    l = xrow_sum(l);
#pragma unroll
    for (int i = 0; i < 4; ++i) L.mrg[sw][8 + hl * 128 + 8 * (4 * g4 + i) + (lane & 7)] = acc[i];
    if ((lane & 55) == 0) *(f32x2*)&L.mrg[sw][hl * 2] = f32x2{m, l};
    unsigned arrived;
    asm volatile("s_mov_b64 exec, 1\n\tds_add_rtn_u32 %0, %1, %2 offset:16\n\ts_mov_b64 exec, -1\n\ts_waitcnt lgkmcnt(0)"
                 : "=v"(arrived) : "v"(sy_addr), "v"(one) : "memory");
    if (__builtin_amdgcn_readfirstlane(arrived) == 3u) {
      const int hh = lane >> 5;
      while (lds_peek(sy_addr + 12) != 4u) __builtin_amdgcn_s_sleep(4);
      float M = -INFINITY;
      f32x2 ml[4];
#pragma unroll
      for (int ww = 0; ww < 4; ++ww) { ml[ww] = *(const f32x2*)&L.mrg[ww][hh * 2]; M = fmaxf(M, ml[ww][0]); }
      float Ls = 0.f;
      f32x4 o = {0.f, 0.f, 0.f, 0.f};
#pragma unroll
      for (int ww = 0; ww < 4; ++ww) {
        const float wt = __builtin_amdgcn_exp2f((ml[ww][0] - M) * C2);
        Ls = fmaf(wt, ml[ww][1], Ls);
        o += wt * *(const f32x4*)&L.mrg[ww][8 + 4 * lane];
      }
      o *= __builtin_amdgcn_rcpf(Ls);
      const unsigned lse = __float_as_uint(fmaf(M, C2, __builtin_amdgcn_logf(Ls))) | 1u;
      const int po = WS_P1A + (((2 * kvh + hh) * NB + b) * NSPLIT + split) * 1024 + (lane & 31) * 32;
      __builtin_amdgcn_raw_buffer_store_b128(u32x4{__float_as_uint(o[0]), lse, __float_as_uint(o[1]), lse}, rs, po, 0, AUX_SC1);
      __builtin_amdgcn_raw_buffer_store_b128(u32x4{__float_as_uint(o[2]), lse, __float_as_uint(o[3]), lse}, rs, po + 16, 0, AUX_SC1);
    }
    STAMP(6);
    {
      const int hh = lane & 31, u = 2 * sw + (lane >> 5);
      const int off = WS_P2 + ((cg * HQ + hh) * 288 + bo * 72 + 9 * (h & 7)) * 4;
      while (lds_peek(sy_addr + 20) < 4u) __builtin_amdgcn_s_sleep(4);
      float y = bf2f(b2[0]), y8 = y;
#pragma unroll
      for (int ww = 0; ww < 4; ++ww) { y += L.red2[ww][u]; y8 += L.red2[ww][8]; }
      float gate = y * __builtin_amdgcn_rcpf(1.0f + __expf(-y)), gate8 = y8 * __builtin_amdgcn_rcpf(1.0f + __expf(-y8));
      float xres = bf2f(x[bo * DM + d0 + u]), xres8 = bf2f(x[bo * DM + d0 + 8]);
      asm volatile("" : "+v"(gate), "+v"(gate8), "+v"(xres), "+v"(xres8));
      unsigned g, g8 = 1u;
      while (lds_peek(sy_addr + 20) < 4u + H2GATE) __builtin_amdgcn_s_sleep(2);
#pragma nounroll
      for (unsigned spins = 0; spins < SPIN_CAP; ++spins) {
        asm volatile("" ::: "memory");
        g = __builtin_amdgcn_raw_buffer_load_b32(rs, off + u * 4, 0, AUX_SC1);
        if (sw == 0) g8 = __builtin_amdgcn_raw_buffer_load_b32(rs, off + 32, 0, AUX_SC1);
        if (__all(g & g8 & 1u)) break;
        __builtin_amdgcn_s_sleep(H2RETRY);
      }
      STAMP(7);
      const float s = x16_sum(row_sum(__uint_as_float(g)));
      if (hh == 0) out[bo * DM + d0 + u] = (unsigned short)(cvtpk(xres + s * gate, 0.f) & 0xffffu);
      if (sw == 0) {
        const float s8 = x16_sum(row_sum(__uint_as_float(g8)));
        if (lane == 0) {
          out[bo * DM + d0 + 8] = (unsigned short)(cvtpk(xres8 + s8 * gate8, 0.f) & 0xffffu);
          if (jb == 0) __builtin_amdgcn_raw_buffer_store_b64(*(const u32x2*)&L.sy[6], rs, WS_E, 0, AUX_SC1);
        }
      }
    }
    STAMP(8);
  }
}

extern "C" void kernel_launch(void* const* d_in, const int* in_sizes, int n_in, void* d_out, int out_size, void* d_ws, size_t ws_size,
                              hipStream_t stream) {
  k_fused<<<NWG, NTHR, 0, stream>>>((const unsigned short*)d_in[0], (const unsigned short*)d_in[1], (const unsigned short*)d_in[2],
                                    (const unsigned short*)d_in[3], (const unsigned short*)d_in[4], (const unsigned short*)d_in[6],
                                    (const unsigned short*)d_in[7], (const unsigned short*)d_in[8], (const unsigned short*)d_in[9],
                                    (unsigned char*)d_ws, (unsigned short*)d_out);
}
```

```cpp
#include <hip/hip_runtime.h>
#include <hip/hip_bf16.h>
#include <stdint.h>
#pragma clang diagnostic ignored "-Winline-asm"

#define GAS __attribute__((address_space(1)))
typedef unsigned u32x4 __attribute__((ext_vector_type(4)));
typedef unsigned u32x2 __attribute__((ext_vector_type(2)));
typedef float f32x4 __attribute__((ext_vector_type(4)));
typedef float f32x2 __attribute__((ext_vector_type(2)));
typedef short bf16x8 __attribute__((ext_vector_type(8)));
typedef __bf16 bf16x2 __attribute__((ext_vector_type(2)));
typedef unsigned long long u64;

constexpr int NB = 4, DM = 576, HQ = 32, HKV = 16, SK = 8192, DH = 128, FF = 4608;
constexpr int NWG = 256, NW = 8, NTHR = 512;
#define DEPTH 4
#define NL 2
#define ROT 1
#define QSLEEP 3
#define QRETRY 8
#define H1GAP 4
#define H2GAP 3
#define H1P 1
#define H2P 2
#define H2GATE 4
#define STAG0 8
#define FLAGAT 24
constexpr int NSPLIT = 4, KEYS_WG = SK / NSPLIT;
constexpr int NT = KEYS_WG / 64;
constexpr int NR = NT - NL;
constexpr float C2 = 0.088388347648318440f * 1.4426950408889634f;
constexpr unsigned SPIN_CAP = 1u << 16;

constexpr int WS_E = 0;
constexpr int WS_FL = 4096;
constexpr int WS_Q0 = 8192;
constexpr int WS_P1 = WS_Q0 + NB * HKV * 4 * 256 * 8;
constexpr int P1B = 1152;
constexpr int WS_P2 = WS_P1 + HQ * NB * NSPLIT * P1B;
constexpr int WS_Y = WS_P2 + 8 * HQ * NB * 72 * 4;
constexpr int WS_END = WS_Y + NB * DM * 4 * 8;
constexpr int WS_STAMP = 4 << 20;
constexpr int AUX_SC1 = 16;

__device__ __forceinline__ float bf2f(unsigned short v) { return __builtin_bit_cast(float, (unsigned)v << 16); }
__device__ __forceinline__ float bflo(unsigned v) { return __builtin_bit_cast(float, v << 16); }
__device__ __forceinline__ float bfhi(unsigned v) { return __builtin_bit_cast(float, v & 0xffff0000u); }
__device__ __forceinline__ unsigned cvtpk(float lo, float hi) {
  bf16x2 r = {(__bf16)lo, (__bf16)hi};
  return __builtin_bit_cast(unsigned, r);
}
__device__ __forceinline__ float dot2(unsigned k, unsigned q, float acc) {
  return __builtin_amdgcn_fdot2_f32_bf16(__builtin_bit_cast(bf16x2, k), __builtin_bit_cast(bf16x2, q), acc, false);
}
template <int CTRL> __device__ __forceinline__ float dpp(float x) {
  return __builtin_bit_cast(float, __builtin_amdgcn_mov_dpp(__builtin_bit_cast(int, x), CTRL, 0xf, 0xf, true));
}
constexpr int XOR1 = 0xB1, XOR2 = 0x4E, XOR7 = 0x141, ROR8 = 0x128;
__device__ __forceinline__ float x16_sum(float x) {
  auto s = __builtin_amdgcn_permlane16_swap(__float_as_uint(x), __float_as_uint(x), false, false);
  return __uint_as_float(s[0]) + __uint_as_float(s[1]);
}
__device__ __forceinline__ float xrow_max(float x) {
  auto s = __builtin_amdgcn_permlane16_swap(__float_as_uint(x), __float_as_uint(x), false, false);
  x = fmaxf(__uint_as_float(s[0]), __uint_as_float(s[1]));
  auto t = __builtin_amdgcn_permlane32_swap(__float_as_uint(x), __float_as_uint(x), false, false);
  return fmaxf(__uint_as_float(t[0]), __uint_as_float(t[1]));
}
__device__ __forceinline__ float xrow_sum(float x) {
  x = x16_sum(x);
  auto t = __builtin_amdgcn_permlane32_swap(__float_as_uint(x), __float_as_uint(x), false, false);
  return __uint_as_float(t[0]) + __uint_as_float(t[1]);
}
__device__ __forceinline__ float row_sum(float x) {
  x += dpp<XOR1>(x); x += dpp<XOR2>(x); x += dpp<XOR7>(x); x += dpp<ROR8>(x);
  return x;
}
__device__ __forceinline__ float wave_sum(float x) { return xrow_sum(row_sum(x)); }
__device__ __forceinline__ f32x4 mfma16(u32x4 a, u32x4 b, f32x4 c) {
  asm volatile("s_nop 1\n\tv_mfma_f32_16x16x32_bf16 %0, %1, %2, %0\n\ts_nop 7" : "+v"(c) : "v"(a), "v"(b));
  return c;
}
__device__ __forceinline__ u32x4 ldnt(const GAS u32x4* p) { return __builtin_nontemporal_load(p); }
#define SB() do { asm volatile("" ::: "memory"); __builtin_amdgcn_sched_barrier(0); } while (0)
#define ORDER() __builtin_amdgcn_fence(__ATOMIC_ACQUIRE, "wavefront")

#define STAMP(k) do {} while (0)
#define LAS __attribute__((address_space(3)))
#define RING_CLOBBERS "a0", "a1", "a2", "a3", "a4", "a5", "a6", "a7", "a8", "a9", "a10", "a11", "a12", "a13", "a14", "a15", "a16", "a17", "a18", "a19", "a20", "a21", "a22", "a23", "a24", "a25", "a26", "a27", "a28", "a29", "a30", "a31", "a32", "a33", "a34", "a35", "a36", "a37", "a38", "a39", "a40", "a41", "a42", "a43", "a44", "a45", "a46", "a47", "a48", "a49", "a50", "a51", "a52", "a53", "a54", "a55", "a56", "a57", "a58", "a59", "a60", "a61", "a62", "a63", "a64", "a65", "a66", "a67", "a68", "a69", "a70", "a71", "a72", "a73", "a74", "a75", "a76", "a77", "a78", "a79", "a80", "a81", "a82", "a83", "a84", "a85", "a86", "a87", "a88", "a89", "a90", "a91", "a92", "a93", "a94", "a95", "a96", "a97", "a98", "a99", "a100", "a101", "a102", "a103", "a104", "a105", "a106", "a107", "a108", "a109", "a110", "a111", "a112", "a113", "a114", "a115", "a116", "a117", "a118", "a119", "a120", "a121", "a122", "a123", "a124", "a125", "a126", "a127"

struct Lds {
  u32x4 kv[4][NL][8][64];
  unsigned char wt[4][5120];
  unsigned char mw[3][2304];
  unsigned short q[256];
  float st1[NB][2];
  float gate[16];
  __attribute__((aligned(16))) unsigned char pr[2][1024];
  __attribute__((aligned(16))) unsigned pl[2][4];
  __attribute__((aligned(16))) unsigned sy[12];
};
static_assert(DEPTH <= 4, "the ring lives in a[0:127]");
constexpr int VMW = 4 + 8 * (DEPTH - 1);

template <int SL> __device__ __forceinline__ void step_qk(f32x4& S, int soff, const u32x4 (&qf)[4], u32x4 rk, int koff) {
  asm volatile(
      "s_waitcnt vmcnt(%c9)\n\t"
      "v_mfma_f32_16x16x32_bf16 %0, a[%c1+0:%c1+3], %2, 0\n\t"
      "v_mfma_f32_16x16x32_bf16 %0, a[%c1+4:%c1+7], %3, %0\n\t"
      "v_mfma_f32_16x16x32_bf16 %0, a[%c1+8:%c1+11], %4, %0\n\t"
      "v_mfma_f32_16x16x32_bf16 %0, a[%c1+12:%c1+15], %5, %0\n\t"
      "buffer_load_dwordx4 a[%c1+0:%c1+3], %7, %6, %8 offen nt\n\t"
      "buffer_load_dwordx4 a[%c1+4:%c1+7], %7, %6, %8 offen offset:64 nt\n\t"
      "buffer_load_dwordx4 a[%c1+8:%c1+11], %7, %6, %8 offen offset:128 nt\n\t"
      "buffer_load_dwordx4 a[%c1+12:%c1+15], %7, %6, %8 offen offset:192 nt\n\t"
      "s_nop 4"
      : "=&v"(S)
      : "i"(32 * SL), "v"(qf[0]), "v"(qf[1]), "v"(qf[2]), "v"(qf[3]), "s"(rk), "v"(koff), "s"(soff), "i"(VMW)
      : "memory", RING_CLOBBERS);
}
template <int SL> __device__ __forceinline__ void step_pv(f32x4& acc, const u32x4 (&bb)[4], u32x4 rv, int voff, int soff) {
  asm volatile(
      "s_waitcnt vmcnt(%c9)\n\t"
      "s_nop 0\n\t"
      "v_mfma_f32_16x16x32_bf16 %0, a[%c1+16:%c1+19], %2, %0\n\t"
      "v_mfma_f32_16x16x32_bf16 %0, a[%c1+20:%c1+23], %3, %0\n\t"
      "v_mfma_f32_16x16x32_bf16 %0, a[%c1+24:%c1+27], %4, %0\n\t"
      "v_mfma_f32_16x16x32_bf16 %0, a[%c1+28:%c1+31], %5, %0\n\t"
      "buffer_load_dwordx4 a[%c1+16:%c1+19], %7, %6, %8 offen nt\n\t"
      "buffer_load_dwordx4 a[%c1+20:%c1+23], %7, %6, %8 offen offset:256 nt\n\t"
      "buffer_load_dwordx4 a[%c1+24:%c1+27], %7, %6, %8 offen offset:512 nt\n\t"
      "buffer_load_dwordx4 a[%c1+28:%c1+31], %7, %6, %8 offen offset:768 nt\n\t"
      "s_nop 4"
      : "+v"(acc)
      : "i"(32 * SL), "v"(bb[0]), "v"(bb[1]), "v"(bb[2]), "v"(bb[3]), "s"(rv), "v"(voff), "s"(soff), "i"(VMW)
      : "memory", RING_CLOBBERS);
}
__device__ __forceinline__ void lds_fill(u32x4 rk, u32x4 rv, int koff, int voff, int soff, unsigned dst) {
  unsigned t;
  asm volatile(
      "s_mov_b32 m0, %6\n\ts_add_u32 %0, %6, 960\n\t"
      "buffer_load_dwordx4 %1, %3, %5 offen nt lds\n\t"
      "s_mov_b32 m0, %0\n\ts_add_u32 %0, %0, 960\n\t"
      "buffer_load_dwordx4 %1, %3, %5 offen offset:64 nt lds\n\t"
      "s_mov_b32 m0, %0\n\ts_add_u32 %0, %0, 960\n\t"
      "buffer_load_dwordx4 %1, %3, %5 offen offset:128 nt lds\n\t"
      "s_mov_b32 m0, %0\n\ts_add_u32 %0, %6, 4096\n\t"
      "buffer_load_dwordx4 %1, %3, %5 offen offset:192 nt lds\n\t"
      "s_mov_b32 m0, %0\n\ts_add_u32 %0, %0, 768\n\t"
      "buffer_load_dwordx4 %2, %4, %5 offen nt lds\n\t"
      "s_mov_b32 m0, %0\n\ts_add_u32 %0, %0, 768\n\t"
      "buffer_load_dwordx4 %2, %4, %5 offen offset:256 nt lds\n\t"
      "s_mov_b32 m0, %0\n\ts_add_u32 %0, %0, 768\n\t"
      "buffer_load_dwordx4 %2, %4, %5 offen offset:512 nt lds\n\t"
      "s_mov_b32 m0, %0\n\ts_nop 0\n\t"
      "buffer_load_dwordx4 %2, %4, %5 offen offset:768 nt lds"
      : "=&s"(t) : "v"(koff), "v"(voff), "s"(rk), "s"(rv), "s"(soff), "s"(dst) : "memory", "scc", "m0");
}
__device__ __forceinline__ void dma16(const void* g, unsigned dst) {
  asm volatile("s_mov_b32 m0, %1\n\ts_nop 0\n\tglobal_load_lds_dwordx4 %0, off nt" :: "v"(g), "s"(dst) : "memory", "m0");
}
__device__ __forceinline__ void dma16d(const void* g, unsigned dst) {
  asm volatile("s_mov_b32 m0, %1\n\ts_nop 0\n\tglobal_load_lds_dwordx4 %0, off" :: "v"(g), "s"(dst) : "memory", "m0");
}
__device__ __forceinline__ void mfma4(f32x4& d, const u32x4 (&a)[4], const u32x4 (&b)[4]) {
  asm volatile("s_nop 1\n\t"
               "v_mfma_f32_16x16x32_bf16 %0, %1, %5, %0\n\tv_mfma_f32_16x16x32_bf16 %0, %2, %6, %0\n\t"
               "v_mfma_f32_16x16x32_bf16 %0, %3, %7, %0\n\tv_mfma_f32_16x16x32_bf16 %0, %4, %8, %0\n\ts_nop 7"
               : "+v"(d) : "v"(a[0]), "v"(a[1]), "v"(a[2]), "v"(a[3]), "v"(b[0]), "v"(b[1]), "v"(b[2]), "v"(b[3]));
}
typedef short s16x4 __attribute__((ext_vector_type(4)));
__device__ __forceinline__ u32x4 tr_frag(const unsigned char* p, int step) {
  const u32x2 lo = __builtin_bit_cast(u32x2, __builtin_amdgcn_ds_read_tr16_b64_v4i16((LAS s16x4*)p));
  const u32x2 hi = __builtin_bit_cast(u32x2, __builtin_amdgcn_ds_read_tr16_b64_v4i16((LAS s16x4*)(p + step)));
  return u32x4{lo[0], lo[1], hi[0], hi[1]};
}
__device__ __forceinline__ u32x4 make_rsrc(const void* p, unsigned bytes) {
  const u64 a = (u64)p;
  u32x4 r = {(unsigned)__builtin_amdgcn_readfirstlane((unsigned)a), (unsigned)__builtin_amdgcn_readfirstlane((unsigned)(a >> 32) & 0xffffu), bytes, 0x00020000u};
  return r;
}
#define NTS ""
#define LD16(dst, p, OFF) asm volatile("global_load_dwordx4 %0, %1, off offset:%c2" NTS : "=v"(dst) : "v"(p), "i"(OFF) : "memory")
#define LDU16(dst, p, OFF) asm volatile("global_load_ushort %0, %1, off offset:%c2" NTS : "=v"(dst) : "v"(p), "i"(OFF) : "memory")
#define LD4(dst, p, OFF) asm volatile("global_load_dword %0, %1, off offset:%c2" NTS : "=v"(dst) : "v"(p), "i"(OFF) : "memory")
#define TIE8(a) asm volatile("" : "+v"(a[0]), "+v"(a[1]), "+v"(a[2]), "+v"(a[3]), "+v"(a[4]), "+v"(a[5]), "+v"(a[6]), "+v"(a[7]))

__device__ __forceinline__ unsigned lds_peek(unsigned addr) {
  unsigned f;
  asm volatile("ds_read_b32 %0, %1\n\ts_waitcnt lgkmcnt(0)" : "=v"(f) : "v"(addr) : "memory");
  return __builtin_amdgcn_readfirstlane(f);
}
__device__ __forceinline__ void lds_arrive(unsigned addr, unsigned one) {
  asm volatile("s_mov_b64 exec, 1\n\tds_add_u32 %0, %1\n\ts_mov_b64 exec, -1" :: "v"(addr), "v"(one) : "memory");
}

__global__ void __launch_bounds__(NTHR, 2)
k_fused(const unsigned short* __restrict__ x, const unsigned short* __restrict__ kc, const unsigned short* __restrict__ vc,
        const unsigned short* __restrict__ wq, const unsigned short* __restrict__ wo, const unsigned short* __restrict__ w1,
        const unsigned short* __restrict__ b1, const unsigned short* __restrict__ w2, const unsigned short* __restrict__ b2,
        unsigned char* ws, unsigned short* __restrict__ out) {
  __shared__ __attribute__((aligned(16))) Lds L;
  const int tid = threadIdx.x, lane = tid & 63, w = __builtin_amdgcn_readfirstlane(tid >> 6);
  STAMP(0);
  const int jb = blockIdx.x;
  const int kvh = 2 * (jb & 7) + ((jb >> 3) & 1), gi = jb >> 4;
  const int b = gi >> 2, split = gi & 3;
  const int h = 2 * kvh + (gi >> 3), cg = gi & 7;
  const int bo = h >> 3, d0 = 72 * cg + 9 * (h & 7);
  const int c = lane & 15, hl = c >> 3, g4 = lane >> 4;
  const int sw = w - 4, rot = (ROT * (jb >> 3)) & 31;
#define TOFF(n) (((((n) + rot) & 31) * 4 + sw) * 4096)
  const size_t kvbase = ((size_t)(b * HKV + kvh) * SK + (size_t)split * KEYS_WG) * DH;
  const u32x4 rk = make_rsrc(kc + kvbase, KEYS_WG * DH * 2), rv = make_rsrc(vc + kvbase, KEYS_WG * DH * 2);
  const int koff = c * 256 + g4 * 16, voff = g4 * 1024 + c * 16;
  const unsigned kv_addr = (unsigned)(size_t)(LAS u32x4*)&L.kv[0][0][0][0] + sw * (NL * 8192);
  if (w >= 4) {
    asm volatile("s_nop 4");
    lds_fill(rk, rv, koff, voff, TOFF(0), kv_addr);
  }
  const __amdgpu_buffer_rsrc_t rs = __builtin_amdgcn_make_buffer_rsrc((void*)ws, 0, WS_END, 0x00020000);
  const u32x4 rw = make_rsrc(ws, WS_END);
  const unsigned sy_addr = (unsigned)(size_t)(LAS unsigned*)&L.sy[0];
  const unsigned one = 1u;
  const bool xact = lane < 36;
#define UNPACK16(f, v0, v1) do { _Pragma("unroll") for (int k = 0; k < 4; ++k) { \
    f[2 * k] = bflo(v0[k]); f[2 * k + 1] = bfhi(v0[k]); f[8 + 2 * k] = bflo(v1[k]); f[8 + 2 * k + 1] = bfhi(v1[k]); } } while (0)
#define LN_STATS(f, mean, rstd) do { float s_ = 0.f; _Pragma("unroll") for (int k = 0; k < 16; ++k) s_ += f[k]; \
    mean = wave_sum(xact ? s_ : 0.f) * (1.f / DM); float q_ = 0.f; \
    _Pragma("unroll") for (int k = 0; k < 16; ++k) { f[k] -= mean; q_ = fmaf(f[k], f[k], q_); } \
    rstd = __builtin_amdgcn_rsqf(wave_sum(xact ? q_ : 0.f) * (1.f / DM) + 1e-5f); } while (0)

  if (__builtin_expect(w < 4, 1)) {
    __builtin_amdgcn_s_setprio(3);
    const unsigned wt_addr = (unsigned)(size_t)(LAS unsigned char*)&L.wt[w][0];
    u32x2 e1;
    unsigned tq_lo, tq_hi;
    {
      u32x4 xa[5], xv0, xv1;
      u32x2 ev;
      const int r0 = 144 * (gi >> 2);
      {
        const unsigned short* xr = x + w * DM + 8 * (xact ? lane : 0);
        LD16(xv0, xr, 0); LD16(xv1, xr, 576);
        const unsigned short* wp = wq + (size_t)(r0 + 36 * w + (lane >> 3)) * 4096 + 256 * kvh + 64 * (gi & 3) + 8 * (lane & 7);
        const unsigned short* wp2 = wq + (size_t)(r0 + 36 * w + 32 + ((lane >> 3) & 3)) * 4096 + 256 * kvh + 64 * (gi & 3) + 8 * (lane & 7);
        dma16(wp, wt_addr); dma16(wp + 8 * 4096, wt_addr + 1024); dma16(wp + 16 * 4096, wt_addr + 2048);
        dma16(wp + 24 * 4096, wt_addr + 3072); dma16(wp2, wt_addr + 4096);
        const unsigned short* xp = x + (c >> 2) * DM + r0 + 8 * g4;
        const unsigned short* xp2 = x + (c >> 2) * DM + r0 + 36 * g4 + 32;
        LD16(xa[0], xp, 0); LD16(xa[1], xp, 72); LD16(xa[2], xp, 144); LD16(xa[3], xp, 216);
        asm volatile("global_load_dwordx2 %0, %1, off" : "=v"(*(u32x2*)&xa[4]) : "v"(xp2) : "memory");
        asm volatile("global_load_dwordx2 %0, %1, off sc1" : "=v"(ev) : "v"(ws + WS_E) : "memory");
      }
      STAMP(1);
      if (tid < 3) *(u32x4*)&L.sy[4 * tid] = u32x4{0u, 0u, 0u, 0u};
      asm volatile("s_waitcnt lgkmcnt(0)\n\ts_barrier\n\ts_waitcnt vmcnt(11)" : "+v"(xv0), "+v"(xv1) :: "memory");
      {
        float f[16], mean, rstd;
        UNPACK16(f, xv0, xv1);
        LN_STATS(f, mean, rstd);
        if (lane == 0) *(f32x2*)&L.st1[w][0] = f32x2{mean, rstd};
      }
      asm volatile("s_waitcnt vmcnt(0)" : "+v"(xa[0]), "+v"(xa[1]), "+v"(xa[2]), "+v"(xa[3]), "+v"(*(u32x2*)&xa[4]), "+v"(ev) :: "memory");
      lds_arrive(sy_addr + 4, one);
      STAMP(2);
      u32x4 a[4], a4;
#pragma unroll
      for (int s = 0; s < 4; ++s)
#pragma unroll
        for (int k = 0; k < 4; ++k) a[s][k] = (c & 3) == 0 ? xa[s][k] : ((c & 3) == 1 ? 0x3F803F80u : 0u);
#pragma unroll
      for (int k = 0; k < 4; ++k) a4[k] = k >= 2 ? 0u : (c & 3) == 0 ? xa[4][k] : ((c & 3) == 1 ? 0x3F803F80u : 0u);
      const u64 e1w = (((u64)ev[1] << 32) | ev[0]) + 1ull;
      e1 = u32x2{(unsigned)e1w, (unsigned)(e1w >> 32)};
      tq_lo = e1[0] & 0xffu; tq_hi = (unsigned)(e1w >> 8);
      const int po = WS_Q0 + (((g4 * HKV + kvh) * 4 + (gi >> 2)) * 256 + 64 * (gi & 3) + 16 * w + c) * 8;
      const int qoff = WS_Q0 + ((b * HKV + kvh) * 1024 + 64 * w + lane) * 8;
      asm volatile("" : "+v"(a[0]), "+v"(a[1]), "+v"(a[2]), "+v"(a[3]), "+v"(a4) : "v"(po), "v"(qoff), "v"(tq_lo), "v"(tq_hi));
      while (lds_peek(sy_addr + 4) != 4u) {}
      f32x4 acc = {0.f, 0.f, 0.f, 0.f};
      {
        const unsigned char* tp = &L.wt[0][(8 * g4 + (c >> 2)) * 128 + 32 * w + (c & 3) * 8];
        const unsigned char* tp2 = &L.wt[g4][(32 + (c >> 2)) * 128 + 32 * w + (c & 3) * 8];
        u32x4 bb[4];
#pragma unroll
        for (int s = 0; s < 4; ++s) bb[s] = tr_frag(tp + 5120 * s, 512);
        const u32x2 lo = __builtin_bit_cast(u32x2, __builtin_amdgcn_ds_read_tr16_b64_v4i16((LAS s16x4*)tp2));
        mfma4(acc, a, bb);
        acc = mfma16(a4, u32x4{lo[0], lo[1], 0u, 0u}, acc);
      }
      const f32x2 st = *(const f32x2*)&L.st1[g4][0];
      const float qv = (acc[0] - st[0] * acc[1]) * st[1];
      __builtin_amdgcn_raw_buffer_store_b64(u32x2{((__float_as_uint(qv) + 0x80u) & 0xffffff00u) | tq_lo, tq_hi}, rs, po, 0, AUX_SC1);
      STAMP(3);
      u32x2 g[4];
      for (int i = 0; i < QSLEEP; ++i) __builtin_amdgcn_s_sleep(16);
#pragma nounroll
      for (unsigned spins = 0; spins < SPIN_CAP; ++spins) {
        asm volatile("" ::: "memory");
#pragma unroll
        for (int k = 0; k < 4; ++k) g[k] = __builtin_amdgcn_raw_buffer_load_b64(rs, qoff + 2048 * k, 0, AUX_SC1);
        unsigned bad = 0;
#pragma unroll
        for (int k = 0; k < 4; ++k) bad |= ((g[k][0] ^ tq_lo) & 0xffu) | (g[k][1] ^ tq_hi);
        if (__all(bad == 0)) break;
        __builtin_amdgcn_s_sleep(QRETRY);
      }
      float qs = 0.f;
#pragma unroll
      for (int k = 0; k < 4; ++k) qs += __uint_as_float(g[k][0] & 0xffffff00u);
      L.q[64 * w + lane] = (unsigned short)(cvtpk(qs, 0.f) & 0xffffu);
      lds_arrive(sy_addr + 8, one);
      STAMP(4);
    }
    __builtin_amdgcn_s_setprio(0);
    if (w == 0) *(u32x2*)&L.sy[6] = e1;
    for (int i = 0; i < STAG0; ++i) __builtin_amdgcn_s_sleep(16);
    while (lds_peek(sy_addr + 8) != 4u) __builtin_amdgcn_s_sleep(4);
    __builtin_amdgcn_s_setprio(3);
    {
      const u32x4 z = {0u, 0u, 0u, 0u};
      if (w < 2) {
        const int o = WS_P1 + (((2 * kvh + w) * NB + b) * NSPLIT + split) * P1B + lane * 16;
        __builtin_amdgcn_raw_buffer_store_b128(z, rs, o, 0, AUX_SC1);
        if (lane < 8) __builtin_amdgcn_raw_buffer_store_b128(z, rs, o + 1024, 0, AUX_SC1);
      } else {
        __builtin_amdgcn_raw_buffer_store_b128(z, rs, WS_P2 + (cg * HQ + h) * 1152 + (w - 2) * 128 + lane * 16, 0, AUX_SC1);
      }
    }
    {
      const int dq = 72 * cg + 36 * ((h >> 2) & 1) + 9 * w;
      u32x4 xv0, xv1;
      unsigned xs;
      {
        const unsigned short* xr = x + bo * DM + 8 * (xact ? lane : 0);
        LD16(xv0, xr, 0); LD16(xv1, xr, 576);
        LDU16(xs, x + bo * DM + dq + (lane < 9 ? lane : 0), 0);
        if (w < 3) {
          const unsigned short* mp = (w == 0 ? w1 : w == 1 ? b1 : w2) + 1152 * (h & 3) + 8 * lane;
          const unsigned ma = (unsigned)(size_t)(LAS unsigned char*)&L.mw[0][0] + 2304 * w;
          dma16d(mp, ma); dma16d(mp + 512, ma + 1024);
          if (lane < 16) dma16d(mp + 1024, ma + 2048);
        }
      }
      {
        const unsigned short* wop = wo + (size_t)(128 * h + 32 * w) * DM + 72 * cg;
#pragma unroll
        for (int t = 0; t < 5; ++t) {
          const int i = t < 4 ? 64 * t + lane : 256 + (lane & 31);
          const int row = (i * 7282) >> 16;
          dma16(wop + row * DM + 8 * (i - 9 * row), wt_addr + 1024 * t);
        }
      }
      __builtin_amdgcn_s_setprio(0);
      asm volatile("s_waitcnt vmcnt(5)" : "+v"(xv0), "+v"(xv1), "+v"(xs) :: "memory");
      lds_arrive(sy_addr + 32, one);
      STAMP(5);
      float hv[9];
      {
        float f[16], mean, rstd, mean2, rstd2;
        UNPACK16(f, xv0, xv1);
        LN_STATS(f, mean, rstd);
        u32x4 o0, o1;
#pragma unroll
        for (int k = 0; k < 4; ++k) { o0[k] = cvtpk(f[2 * k] * rstd, f[2 * k + 1] * rstd); o1[k] = cvtpk(f[8 + 2 * k] * rstd, f[8 + 2 * k + 1] * rstd); }
        UNPACK16(f, o0, o1);
        LN_STATS(f, mean2, rstd2);
        const float xn = bflo(cvtpk((bflo(xs) - mean) * rstd, 0.f));
        const unsigned hb = cvtpk(0.f, (xn - mean2) * rstd2);
#pragma unroll
        for (int u = 0; u < 9; ++u) hv[u] = __builtin_bit_cast(float, __builtin_amdgcn_readlane(hb, u));
      }
      while (lds_peek(sy_addr + 32) != 4u) __builtin_amdgcn_s_sleep(4);
      u32x4 m1v[2], mbv[2], m2v[2];
#pragma unroll
      for (int k = 0; k < 2; ++k) {
        m1v[k] = *(const u32x4*)&L.mw[0][1024 * k + 16 * lane];
        mbv[k] = *(const u32x4*)&L.mw[1][1024 * k + 16 * lane];
        m2v[k] = *(const u32x4*)&L.mw[2][1024 * k + 16 * lane];
      }
      const unsigned m1s = *(const unsigned*)&L.mw[0][2048 + 4 * lane], mbs = *(const unsigned*)&L.mw[1][2048 + 4 * lane],
                     m2s = *(const unsigned*)&L.mw[2][2048 + 4 * lane];
      constexpr float G0 = -2.0f * 0.7978845608028654f * 1.4426950408889634f, G1 = G0 * 0.044715f;
      f32x2 ya[9];
#pragma unroll
      for (int u = 0; u < 9; ++u) ya[u] = f32x2{0.f, 0.f};
#pragma unroll
      for (int ms = 0; ms < 9; ++ms) {
        const unsigned r1 = ms < 8 ? m1v[ms >> 2][ms & 3] : m1s, rb = ms < 8 ? mbv[ms >> 2][ms & 3] : mbs, r2 = ms < 8 ? m2v[ms >> 2][ms & 3] : m2s;
        const f32x2 A1 = {bflo(r1), bfhi(r1)}, C1 = {bflo(rb), bfhi(rb)}, A2 = {bflo(r2), bfhi(r2)};
#pragma unroll
        for (int u = 0; u < 9; ++u) {
          const f32x2 uu = A1 * hv[u] + C1;
          const f32x2 z = uu * (uu * uu * G1 + G0);
          const f32x2 e = f32x2{__builtin_amdgcn_exp2f(z[0]), __builtin_amdgcn_exp2f(z[1])} + 1.0f;
          const f32x2 gl = uu * f32x2{__builtin_amdgcn_rcpf(e[0]), __builtin_amdgcn_rcpf(e[1])};
          ya[u] = gl * A2 + ya[u];
        }
      }
      float yv = 0.f;
#pragma unroll
      for (int u = 0; u < 9; ++u) { const float s = wave_sum(ya[u][0] + ya[u][1]); yv = lane == u ? s : yv; }
      __builtin_amdgcn_s_setprio(3);
      if (lane < 9)
        __builtin_amdgcn_raw_buffer_store_b64(u32x2{((__float_as_uint(yv) + 0x80u) & 0xffffff00u) | tq_lo, tq_hi}, rs,
                                              WS_Y + ((bo * DM + dq + lane) * 4 + (h & 3)) * 8, 0, AUX_SC1);
      asm volatile("s_waitcnt vmcnt(1)" ::: "memory");
      lds_arrive(sy_addr + 12, one);
      STAMP(6);
    }
    while (lds_peek(sy_addr + 12) != 4u) __builtin_amdgcn_s_sleep(8);
    if (tid == 0) __builtin_amdgcn_raw_buffer_store_b64(e1, rs, WS_FL + jb * 8, 0, AUX_SC1);
    u32x4 wf[5][4];
#pragma unroll
    for (int nt = 0; nt < 5; ++nt)
#pragma unroll
      for (int ks = 0; ks < 4; ++ks) wf[nt][ks] = tr_frag(&L.wt[ks][(8 * (c >> 2) + 2 * g4) * 144 + (c & 3) * 8 + 32 * nt], 144);
    while (lds_peek(sy_addr) == 0u) __builtin_amdgcn_s_sleep(32);
    {
      u32x2 yg;
      const bool ya = w == 3 && lane < 36;
      for (unsigned spins = 0; spins < SPIN_CAP; ++spins) {
        asm volatile("" ::: "memory");
        const u32x2 f = __builtin_amdgcn_raw_buffer_load_b64(rs, WS_FL + tid * 8, 0, AUX_SC1);
        yg = __builtin_amdgcn_raw_buffer_load_b64(rs, ya ? WS_Y + ((bo * DM + d0) * 4 + lane) * 8 : 0x40000000, 0, AUX_SC1);
        if (__all(f[0] == e1[0] && f[1] == e1[1] && (!ya || (((yg[0] ^ tq_lo) & 0xffu) | (yg[1] ^ tq_hi)) == 0u))) break;
        __builtin_amdgcn_s_sleep(8);
      }
      if (w == 3) {
        float y = __uint_as_float(yg[0] & 0xffffff00u);
        y += dpp<XOR1>(y); y += dpp<XOR2>(y);
        y += bf2f(b2[0]);
        if ((lane & 3) == 0) L.gate[lane >> 2] = y * __builtin_amdgcn_rcpf(1.0f + __expf(-y));
      }
    }
    lds_arrive(sy_addr + 20, one);
    while (lds_peek(sy_addr + 20) < 4u) __builtin_amdgcn_s_sleep(2);
    u32x4 a[4], ls = {0u, 0u, 0u, 0u};
#pragma unroll
    for (int ks = 0; ks < 4; ++ks) a[ks] = u32x4{0u, 0u, 0u, 0u};
    {
      const int off = WS_P1 + ((h * NB + w) * NSPLIT + (c >> 2)) * P1B + (c & 3) * 256 + 16 * g4;
      const int loff = WS_P1 + ((h * NB + w) * NSPLIT + g4) * P1B + 1024;
      while (lds_peek(sy_addr + 16) != 4u) __builtin_amdgcn_s_sleep(4);
      STAMP(7);
      u64 pd, pl;
      unsigned t0, t1, n;
#define H1_ISSUE \
      "s_mov_b64 exec, %5\n\t" \
      "buffer_load_dwordx4 v[108:111], %10, %12, 0 offen sc1\n\t" \
      "buffer_load_dwordx4 v[112:115], %10, %12, 0 offen offset:64 sc1\n\t" \
      "buffer_load_dwordx4 v[116:119], %10, %12, 0 offen offset:128 sc1\n\t" \
      "buffer_load_dwordx4 v[120:123], %10, %12, 0 offen offset:192 sc1\n\t" \
      "s_mov_b64 exec, %6\n\t" \
      "buffer_load_dwordx4 v[124:127], %11, %12, 0 offen sc1\n\t" \
      "s_mov_b64 exec, -1\n\t"
#define H1_FIRST "s_mov_b64 %5, -1\n\ts_mov_b64 %6, -1\n\t"
      asm volatile(
          H1_FIRST "s_mov_b32 %9, 0\n\t"
          H1_ISSUE
          "s_waitcnt vmcnt(0)\n"
          "1:\n\t"
          "v_min3_u32 %7, v108, v109, v110\n\tv_min3_u32 %8, v111, v112, v113\n\t"
          "v_min3_u32 %7, %7, v114, v115\n\tv_min3_u32 %8, %8, v116, v117\n\t"
          "v_min3_u32 %7, %7, v118, v119\n\tv_min3_u32 %8, %8, v120, v121\n\t"
          "v_min3_u32 %7, %7, v122, v123\n\tv_min_u32 %7, %7, %8\n\t"
          "v_min3_u32 %8, v124, v125, v126\n\tv_min_u32 %8, %8, v127\n\t"
          "v_cmp_eq_u32 %5, 0, %7\n\t"
          "v_cmp_eq_u32 %6, 0, %8\n\t"
          "s_add_u32 %9, %9, 1\n\t"
          "s_cmp_ge_u32 %9, %c13\n\t"
          "s_cbranch_scc1 2f\n\t"
          "s_or_b64 vcc, %5, %6\n\t"
          "s_cbranch_scc0 2f\n\t"
          H1_ISSUE
          "s_sleep %c14\n\t"
          "s_waitcnt vmcnt(%c15)\n\t"
          "s_branch 1b\n"
          "2:"
          : "+{v[108:111]}"(a[0]), "+{v[112:115]}"(a[1]), "+{v[116:119]}"(a[2]), "+{v[120:123]}"(a[3]), "+{v[124:127]}"(ls),
            "=&s"(pd), "=&s"(pl), "=&v"(t0), "=&v"(t1), "=&s"(n)
          : "v"(off), "v"(loff), "s"(rw), "i"(SPIN_CAP), "i"(H1GAP), "i"(5 * (H1P - 1))
          : "memory", "scc", "vcc");
      STAMP(8);
    }
    {
      f32x4 a2[5];
#pragma unroll
      for (int nt = 0; nt < 5; ++nt) a2[nt] = f32x4{0.f, 0.f, 0.f, 0.f};
      asm volatile("s_nop 1\n\t"
                   "v_mfma_f32_16x16x32_bf16 %0, %5, %9, %0\n\tv_mfma_f32_16x16x32_bf16 %1, %5, %13, %1\n\tv_mfma_f32_16x16x32_bf16 %2, %5, %17, %2\n\t"
                   "v_mfma_f32_16x16x32_bf16 %3, %5, %21, %3\n\tv_mfma_f32_16x16x32_bf16 %4, %5, %25, %4\n\t"
                   "v_mfma_f32_16x16x32_bf16 %0, %6, %10, %0\n\tv_mfma_f32_16x16x32_bf16 %1, %6, %14, %1\n\tv_mfma_f32_16x16x32_bf16 %2, %6, %18, %2\n\t"
                   "v_mfma_f32_16x16x32_bf16 %3, %6, %22, %3\n\tv_mfma_f32_16x16x32_bf16 %4, %6, %26, %4\n\t"
                   "v_mfma_f32_16x16x32_bf16 %0, %7, %11, %0\n\tv_mfma_f32_16x16x32_bf16 %1, %7, %15, %1\n\tv_mfma_f32_16x16x32_bf16 %2, %7, %19, %2\n\t"
                   "v_mfma_f32_16x16x32_bf16 %3, %7, %23, %3\n\tv_mfma_f32_16x16x32_bf16 %4, %7, %27, %4\n\t"
                   "v_mfma_f32_16x16x32_bf16 %0, %8, %12, %0\n\tv_mfma_f32_16x16x32_bf16 %1, %8, %16, %1\n\tv_mfma_f32_16x16x32_bf16 %2, %8, %20, %2\n\t"
                   "v_mfma_f32_16x16x32_bf16 %3, %8, %24, %3\n\tv_mfma_f32_16x16x32_bf16 %4, %8, %28, %4"
                   : "+v"(a2[0]), "+v"(a2[1]), "+v"(a2[2]), "+v"(a2[3]), "+v"(a2[4])
                   : "{v[108:111]}"(a[0]), "{v[112:115]}"(a[1]), "{v[116:119]}"(a[2]), "{v[120:123]}"(a[3]),
                     "a"(wf[0][0]), "a"(wf[0][1]), "a"(wf[0][2]), "a"(wf[0][3]), "a"(wf[1][0]), "a"(wf[1][1]), "a"(wf[1][2]), "a"(wf[1][3]),
                     "a"(wf[2][0]), "a"(wf[2][1]), "a"(wf[2][2]), "a"(wf[2][3]), "a"(wf[3][0]), "a"(wf[3][1]), "a"(wf[3][2]), "a"(wf[3][3]),
                     "a"(wf[4][0]), "a"(wf[4][1]), "a"(wf[4][2]), "a"(wf[4][3]));
      const float M = xrow_max(fmaxf(fmaxf(__uint_as_float(ls[0]), __uint_as_float(ls[1])), fmaxf(__uint_as_float(ls[2]), __uint_as_float(ls[3]))));
      float wv[4];
#pragma unroll
      for (int i = 0; i < 4; ++i) wv[i] = __builtin_amdgcn_exp2f(__uint_as_float(ls[i]) - M);
      const float rl = __builtin_amdgcn_rcpf(xrow_sum((wv[0] + wv[1]) + (wv[2] + wv[3])));
#pragma unroll
      for (int i = 0; i < 4; ++i) wv[i] *= rl;
      asm volatile("s_nop 7" : "+v"(a2[0]), "+v"(a2[1]), "+v"(a2[2]), "+v"(a2[3]), "+v"(a2[4]), "+v"(wv[0]), "+v"(wv[1]), "+v"(wv[2]), "+v"(wv[3]));
      float r[5];
#pragma unroll
      for (int nt = 0; nt < 5; ++nt) r[nt] = fmaf(wv[3], a2[nt][3], fmaf(wv[2], a2[nt][2], fmaf(wv[1], a2[nt][1], wv[0] * a2[nt][0])));
      const auto s01 = __builtin_amdgcn_permlane16_swap(__float_as_uint(r[0]), __float_as_uint(r[1]), false, false);
      const auto s23 = __builtin_amdgcn_permlane16_swap(__float_as_uint(r[2]), __float_as_uint(r[3]), false, false);
      const auto t = __builtin_amdgcn_permlane32_swap(__float_as_uint(__uint_as_float(s01[0]) + __uint_as_float(s01[1])),
                                                      __float_as_uint(__uint_as_float(s23[0]) + __uint_as_float(s23[1])), false, false);
      const int po = WS_P2 + ((cg * HQ + h) * 288 + w * 72 + lane) * 4;
      __builtin_amdgcn_raw_buffer_store_b32(__float_as_uint(__uint_as_float(t[0]) + __uint_as_float(t[1])) | 1u, rs, po, 0, AUX_SC1);
      const float r4 = xrow_sum(r[4]);
      if (lane < 8) __builtin_amdgcn_raw_buffer_store_b32(__float_as_uint(r4) | 1u, rs, po + 256, 0, AUX_SC1);
      lds_arrive(sy_addr + 20, one);
    }
    STAMP(9);
    asm volatile("s_waitcnt vmcnt(0)" : "+{v[108:111]}"(a[0]), "+{v[112:115]}"(a[1]), "+{v[116:119]}"(a[2]), "+{v[120:123]}"(a[3]), "+{v[124:127]}"(ls) :: "memory");
  } else {
    __builtin_amdgcn_s_setprio(1);
#define REC() do {} while (0)
    STAMP(1);
    asm volatile("s_barrier" ::: "memory");
    unsigned msk[4];
#pragma unroll
    for (int k = 0; k < 4; ++k) msk[k] = ((c & 7) >> 1) == k ? ((c & 1) ? 0xffff0000u : 0x0000ffffu) : 0u;
    f32x4 acc = {0.f, 0.f, 0.f, 0.f};
    float m = -INFINITY, mc = 0.f, l = 0.f;
    u32x4 qf[4];
#pragma unroll
    for (int i = 0; i < 4; ++i) qf[i] = u32x4{0u, 0u, 0u, 0u};

#define SOFTMAX(S, bb) do { \
    float mt = fmaxf(fmaxf(S[0], S[1]), fmaxf(S[2], S[3])); \
    if (__any(mt > m)) { \
      mt = xrow_max(mt); \
      const float mn = fmaxf(m, mt); \
      const float alpha = __builtin_amdgcn_exp2f((m - mn) * C2); \
      m = mn; mc = -mn * C2; acc *= alpha; l *= alpha; \
    } \
    _Pragma("unroll") for (int t = 0; t < 4; ++t) { \
      const float p = __builtin_amdgcn_exp2f(fmaf(S[t], C2, mc)); \
      l += p; \
      const unsigned pp = cvtpk(p, p); \
      bb[t] = u32x4{pp & msk[0], pp & msk[1], pp & msk[2], pp & msk[3]}; } } while (0)
#define TILE(SL) do { \
    if (n + SL >= NR + DEPTH) goto done; \
    f32x4 S; u32x4 bb[4]; \
    const int so_ = n + SL < NR ? TOFF(NL + n + SL) : 0x40000000; \
    step_qk<SL>(S, so_, qf, rk, koff); \
    SOFTMAX(S, bb); \
    step_pv<SL>(acc, bb, rv, voff, so_); REC(); } while (0)
#pragma nounroll
    for (int n = 0;; n += DEPTH) {
      TILE(0);
      TILE(1);
      TILE(2);
      TILE(3);
      if (n <= NL * DEPTH) {
        if (n == 0) {
          STAMP(2);
#pragma unroll
          for (int i = 1; i < NL; ++i) lds_fill(rk, rv, koff, voff, TOFF(i), kv_addr + 8192 * i);
          STAMP(3);
          while (lds_peek(sy_addr + 8) != 4u) __builtin_amdgcn_s_sleep(1);
          STAMP(4);
#pragma unroll
          for (int i = 0; i < 4; ++i) qf[i] = *(const u32x4*)&L.q[hl * 128 + 32 * i + 8 * g4];
          acc = f32x4{0.f, 0.f, 0.f, 0.f};
          m = -INFINITY; mc = 0.f; l = 0.f;
        } else {
          asm volatile("s_waitcnt vmcnt(%c0)" :: "i"(8 * DEPTH) : "memory");
          u32x4 kf[4], vf[4], bb[4];
          const u32x4* kp = &L.kv[sw][0][0][lane] + (n / DEPTH - 1) * 512;
#pragma unroll
          for (int i = 0; i < 4; ++i) { kf[i] = kp[64 * i]; vf[i] = kp[256 + 64 * i]; }
          f32x4 S = {0.f, 0.f, 0.f, 0.f};
          mfma4(S, kf, qf);
          SOFTMAX(S, bb);
          mfma4(acc, vf, bb); REC();
        }
      } else if (n == FLAGAT) {
        lds_arrive(sy_addr, one);
        while (lds_peek(sy_addr + 12) != 4u) __builtin_amdgcn_s_sleep(4);
      }
    }
  done:
    STAMP(5);
    {
      const float lt = xrow_sum(l), ri = __builtin_amdgcn_rcpf(lt);
      const unsigned w0 = cvtpk(acc[0] * ri, acc[1] * ri), w1_ = cvtpk(acc[2] * ri, acc[3] * ri);
      *(u32x2*)&L.pr[hl][sw * 256 + (g4 * 8 + (c & 7)) * 8] = u32x2{w0 > 1u ? w0 : 1u, w1_ > 1u ? w1_ : 1u};
      if ((lane & 55) == 0) L.pl[hl][sw] = __float_as_uint(fmaf(m, C2, __builtin_amdgcn_logf(lt))) | 1u;
      unsigned arrived;
      asm volatile("s_mov_b64 exec, 1\n\tds_add_rtn_u32 %0, %1, %2 offset:16\n\ts_mov_b64 exec, -1\n\ts_waitcnt lgkmcnt(0)"
                   : "=v"(arrived) : "v"(sy_addr), "v"(one) : "memory");
      if (__builtin_amdgcn_readfirstlane(arrived) == 3u) {
        const u32x4 r0 = *(const u32x4*)&L.pr[0][lane * 16], r1 = *(const u32x4*)&L.pr[1][lane * 16], rl = *(const u32x4*)&L.pl[(lane >> 3) & 1][0];
        const int o = WS_P1 + ((2 * kvh * NB + b) * NSPLIT + split) * P1B;
        __builtin_amdgcn_raw_buffer_store_b128(r0, rs, o + lane * 16, 0, AUX_SC1);
        __builtin_amdgcn_raw_buffer_store_b128(r1, rs, o + NB * NSPLIT * P1B + lane * 16, 0, AUX_SC1);
        if (lane < 16) __builtin_amdgcn_raw_buffer_store_b128(rl, rs, o + (lane >> 3) * (NB * NSPLIT * P1B) + 1024 + (lane & 7) * 16, 0, AUX_SC1);
      }
    }
    STAMP(9);
    {
      const int hh = lane & 31, u = 2 * sw + (lane >> 5);
      const int off = WS_P2 + ((cg * HQ + hh) * 288 + bo * 72 + 9 * (h & 7)) * 4;
      float xres = bf2f(x[bo * DM + d0 + u]), xres8 = bf2f(x[bo * DM + d0 + 8]);
      while (lds_peek(sy_addr + 20) < 4u) __builtin_amdgcn_s_sleep(4);
      float gate = L.gate[u], gate8 = L.gate[8];
      asm volatile("" : "+v"(gate), "+v"(gate8), "+v"(xres), "+v"(xres8));
      unsigned g = 0u, g8 = sw == 0 ? 0u : 1u;
      while (lds_peek(sy_addr + 20) < 4u + H2GATE) __builtin_amdgcn_s_sleep(2);
      STAMP(6);
      u64 pd, pl;
      unsigned t0, n;
#define H2_ISSUE \
      "s_mov_b64 exec, %2\n\t" \
      "buffer_load_dword %0, %6, %8, 0 offen sc1\n\t" \
      "s_mov_b64 exec, %3\n\t" \
      "buffer_load_dword %1, %7, %8, 0 offen offset:32 sc1\n\t" \
      "s_mov_b64 exec, -1\n\t"
      asm volatile(
          "s_mov_b32 %5, 0\n\t"
          "s_waitcnt vmcnt(0)\n"
          "1:\n\t"
          "v_and_b32 %4, 1, %0\n\t"
          "v_cmp_eq_u32 %2, 0, %4\n\t"
          "v_and_b32 %4, 1, %1\n\t"
          "v_cmp_eq_u32 %3, 0, %4\n\t"
          "s_add_u32 %5, %5, 1\n\t"
          "s_cmp_ge_u32 %5, %c9\n\t"
          "s_cbranch_scc1 2f\n\t"
          "s_or_b64 vcc, %2, %3\n\t"
          "s_cbranch_scc0 2f\n\t"
          H2_ISSUE
          "s_sleep %c10\n\t"
          "s_waitcnt vmcnt(%c11)\n\t"
          "s_branch 1b\n"
          "2:"
          : "+{v126}"(g), "+{v127}"(g8), "=&s"(pd), "=&s"(pl), "=&v"(t0), "=&s"(n)
          : "v"(off + u * 4), "v"(off), "s"(rw), "i"(SPIN_CAP), "i"(H2GAP), "i"(2 * (H2P - 1))
          : "memory", "scc", "vcc");
      STAMP(7);
      const float s = x16_sum(row_sum(__uint_as_float(g)));
      if (hh == 0) out[bo * DM + d0 + u] = (unsigned short)(cvtpk(xres + s * gate, 0.f) & 0xffffu);
      if (sw == 0) {
        const float s8 = x16_sum(row_sum(__uint_as_float(g8)));
        if (lane == 0) {
          out[bo * DM + d0 + 8] = (unsigned short)(cvtpk(xres8 + s8 * gate8, 0.f) & 0xffffu);
          if (jb == 0) __builtin_amdgcn_raw_buffer_store_b64(*(const u32x2*)&L.sy[6], rs, WS_E, 0, AUX_SC1);
        }
      }
      STAMP(8);
      asm volatile("s_waitcnt vmcnt(0)" : "+{v126}"(g), "+{v127}"(g8) :: "memory");
    }
  }
}

extern "C" void kernel_launch(void* const* d_in, const int* in_sizes, int n_in, void* d_out, int out_size, void* d_ws, size_t ws_size,
                              hipStream_t stream) {
  k_fused<<<NWG, NTHR, 0, stream>>>((const unsigned short*)d_in[0], (const unsigned short*)d_in[1], (const unsigned short*)d_in[2],
                                    (const unsigned short*)d_in[3], (const unsigned short*)d_in[4], (const unsigned short*)d_in[6],
                                    (const unsigned short*)d_in[7], (const unsigned short*)d_in[8], (const unsigned short*)d_in[9],
                                    (unsigned char*)d_ws, (unsigned short*)d_out);
}
```

```cpp
#include <hip/hip_runtime.h>
#include <hip/hip_bf16.h>
#include <stdint.h>
#pragma clang diagnostic ignored "-Winline-asm"

#define GAS __attribute__((address_space(1)))
typedef unsigned u32x4 __attribute__((ext_vector_type(4)));
typedef unsigned u32x2 __attribute__((ext_vector_type(2)));
typedef unsigned u32x8 __attribute__((ext_vector_type(8)));
typedef unsigned u32x16 __attribute__((ext_vector_type(16)));
typedef float f32x4 __attribute__((ext_vector_type(4)));
typedef float f32x2 __attribute__((ext_vector_type(2)));
typedef short bf16x8 __attribute__((ext_vector_type(8)));
typedef __bf16 bf16x2 __attribute__((ext_vector_type(2)));
typedef unsigned long long u64;
typedef __bf16 bfv8 __attribute__((ext_vector_type(8)));

constexpr int NB = 4, DM = 576, HQ = 32, HKV = 16, SK = 8192, DH = 128, FF = 4608;
constexpr int NWG = 256, NW = 8, NTHR = 512;
#define H2AT 188
#define DB 90
#define QS2 42
#define H1DELAY 4
#define PFB 27800
#define H1AT 130
#define QRETRY 8
#define H1GAP 4
#define H2GAP 4
#define SP1 1
#define SP2 1
#define H1GATE 4
#define H2GATE 4
#define BURST 8
#define LEAD 480
#define PACE 930
#define STAG 0
#define STAGW 0
#define EV 100
#define EV2 50
#define EV3 0
#define GJ 12
#define GAP 0
#define QABL 4
#define P3I 6
#define P3D 0
#define WL0 5
#define DV 4
#define PREN 8
#define WOAT 1000
constexpr int NSPLIT = 4;
#define MAPV 0x0001, 0x0002, 0x0004, 0x0008, 0x0040, 0x0080, 0x0100, 0x0400, 0x0800, 0x1040, 0x0200, 0x0010, 0x0020, 0x2080, 0x4100
#define SPL0 16
#define SPL1 32
constexpr int MAPM[15] = {MAPV};
#define BM(x, bit, i) ((((x) >> (bit)) & 1) ? MAPM[i] : 0)
#define SP0 SPL0
#define SP1_ SPL1
constexpr float C2 = 0.088388347648318440f * 1.4426950408889634f;
constexpr unsigned SPIN_CAP = 1u << 16;

constexpr int WS_E = 0;
constexpr int WS_T = 2048;
constexpr int WS_Q0 = 4096;
constexpr int WS_P1 = WS_Q0 + HKV * 4 * 16 * 256;
constexpr int P1B = 512, P2B = 384;
constexpr int WS_P2 = WS_P1 + HQ * NB * NSPLIT * P1B;
constexpr int WS_END = 2 << 20;
#define P1OFF(h, b, s) ((512 << 10) + ((h) << 14) + (((h) & 16) << 9) + ((b) * NSPLIT + (s)) * P1B)
#define P2OFF(cg, h, b) ((1 << 20) + (((cg) * 4 + ((h) >> 3)) << 14) + (((b) & 2) << 12) + (((h) & 7) * 2 + ((b) & 1)) * P2B)
constexpr int WS_N = 1024;
#define NAT 3000
#define NEARTH 480
#define NTRY 24
#define NEND 2700
#define NGAP 4
constexpr int AUX_SC1 = 16;
#define QAS 16
#define QAL 16

__device__ __forceinline__ float bf2f(unsigned short v) { return __builtin_bit_cast(float, (unsigned)v << 16); }
__device__ __forceinline__ float bflo(unsigned v) { return __builtin_bit_cast(float, v << 16); }
__device__ __forceinline__ float bfhi(unsigned v) { return __builtin_bit_cast(float, v & 0xffff0000u); }
__device__ __forceinline__ unsigned cvtpk(float lo, float hi) {
  bf16x2 r = {(__bf16)lo, (__bf16)hi};
  return __builtin_bit_cast(unsigned, r);
}
__device__ __forceinline__ float dot2(unsigned k, unsigned q, float acc) {
  return __builtin_amdgcn_fdot2_f32_bf16(__builtin_bit_cast(bf16x2, k), __builtin_bit_cast(bf16x2, q), acc, false);
}
template <int CTRL> __device__ __forceinline__ float dpp(float x) {
  return __builtin_bit_cast(float, __builtin_amdgcn_mov_dpp(__builtin_bit_cast(int, x), CTRL, 0xf, 0xf, true));
}
__device__ __forceinline__ unsigned perm(unsigned hi, unsigned lo, unsigned sel) { return __builtin_amdgcn_perm(hi, lo, sel); }
template <int CTRL> __device__ __forceinline__ unsigned dppu(unsigned x) { return __builtin_amdgcn_mov_dpp(x, CTRL, 0xf, 0xf, true); }
__device__ __forceinline__ unsigned wide_tag(unsigned e) { return e + (e << 16); }
__device__ __forceinline__ unsigned byte_tag(unsigned e) {
  const unsigned l = e & 0xffu;
  return (((e & 0x00ff0000u) + (l << 16)) & 0x00ff0000u) | (((e & 0xff00ff00u) + l * 0x01000100u) & 0xff00ff00u) | l;
}
__device__ __forceinline__ int gcol(int g, int t) { return g < 16 ? 16 * t + g : 32 + 8 * t + g; }
constexpr int XOR1 = 0xB1, XOR2 = 0x4E, XOR7 = 0x141, ROR8 = 0x128;
__device__ __forceinline__ float x16_sum(float x) {
  auto s = __builtin_amdgcn_permlane16_swap(__float_as_uint(x), __float_as_uint(x), false, false);
  return __uint_as_float(s[0]) + __uint_as_float(s[1]);
}
__device__ __forceinline__ float xrow_max(float x) {
  auto s = __builtin_amdgcn_permlane16_swap(__float_as_uint(x), __float_as_uint(x), false, false);
  x = fmaxf(__uint_as_float(s[0]), __uint_as_float(s[1]));
  auto t = __builtin_amdgcn_permlane32_swap(__float_as_uint(x), __float_as_uint(x), false, false);
  return fmaxf(__uint_as_float(t[0]), __uint_as_float(t[1]));
}
__device__ __forceinline__ float xrow_sum(float x) {
  x = x16_sum(x);
  auto t = __builtin_amdgcn_permlane32_swap(__float_as_uint(x), __float_as_uint(x), false, false);
  return __uint_as_float(t[0]) + __uint_as_float(t[1]);
}
__device__ __forceinline__ float row_sum(float x) {
  x += dpp<XOR1>(x); x += dpp<XOR2>(x); x += dpp<XOR7>(x); x += dpp<ROR8>(x);
  return x;
}
__device__ __forceinline__ float wave_sum(float x) { return xrow_sum(row_sum(x)); }
__device__ __forceinline__ f32x4 mfma16(u32x4 a, u32x4 b, f32x4 c) {
  asm volatile("s_nop 1\n\tv_mfma_f32_16x16x32_bf16 %0, %1, %2, %0\n\ts_nop 7" : "+v"(c) : "v"(a), "v"(b));
  return c;
}
__device__ __forceinline__ u32x4 ldnt(const GAS u32x4* p) { return __builtin_nontemporal_load(p); }


#define LAS __attribute__((address_space(3)))
#define RING_CLOBBERS "a0", "a1", "a2", "a3", "a4", "a5", "a6", "a7", "a8", "a9", "a10", "a11", "a12", "a13", "a14", "a15", "a16", "a17", "a18", "a19", "a20", "a21", "a22", "a23", "a24", "a25", "a26", "a27", "a28", "a29", "a30", "a31", "a32", "a33", "a34", "a35", "a36", "a37", "a38", "a39", "a40", "a41", "a42", "a43", "a44", "a45", "a46", "a47", "a48", "a49", "a50", "a51", "a52", "a53", "a54", "a55", "a56", "a57", "a58", "a59", "a60", "a61", "a62", "a63", "a64", "a65", "a66", "a67", "a68", "a69", "a70", "a71", "a72", "a73", "a74", "a75", "a76", "a77", "a78", "a79", "a80", "a81", "a82", "a83", "a84", "a85", "a86", "a87", "a88", "a89", "a90", "a91", "a92", "a93", "a94", "a95", "a96", "a97", "a98", "a99", "a100", "a101", "a102", "a103", "a104", "a105", "a106", "a107", "a108", "a109", "a110", "a111", "a112", "a113", "a114", "a115", "a116", "a117", "a118", "a119", "a120", "a121", "a122", "a123", "a124", "a125", "a126", "a127"

struct Lds {
  u32x4 kv[4][4][4][64];
  f32x4 sc[4][256];
  unsigned char pp[4][8][272];
  unsigned char wt[4][5120];
  unsigned char mw[3][1152];
  __attribute__((aligned(16))) unsigned short q[256];
  float st1[NB][2];
  __attribute__((aligned(16))) float pr[4][2][128];
  __attribute__((aligned(16))) float pm[2][4], pl[2][4];
  __attribute__((aligned(16))) unsigned sy[12];
};

template <int SL> __device__ __forceinline__ void ring_qk(f32x4 (&D)[4], u32x4 qf) {
  asm volatile(
      "s_waitcnt vmcnt(28)\n\t"
      "v_mfma_f32_16x16x32_bf16 %0, a[%c4+0:%c4+3], %5, 0\n\t"
      "v_mfma_f32_16x16x32_bf16 %1, a[%c4+4:%c4+7], %5, 0\n\t"
      "v_mfma_f32_16x16x32_bf16 %2, a[%c4+8:%c4+11], %5, 0\n\t"
      "v_mfma_f32_16x16x32_bf16 %3, a[%c4+12:%c4+15], %5, 0\n\t"
      "s_nop 7"
      : "=&v"(D[0]), "=&v"(D[1]), "=&v"(D[2]), "=&v"(D[3]) : "i"(16 * SL), "v"(qf) : "memory", RING_CLOBBERS);
}
template <int SL, int CNT = 28> __device__ __forceinline__ void ring_pv(f32x4& acc, const u32x4 (&bb)[4]) {
  asm volatile(
      "s_waitcnt vmcnt(%c6)\n\t"
      "s_nop 0\n\t"
      "v_mfma_f32_16x16x32_bf16 %0, a[%c1+0:%c1+3], %2, %0\n\t"
      "v_mfma_f32_16x16x32_bf16 %0, a[%c1+4:%c1+7], %3, %0\n\t"
      "v_mfma_f32_16x16x32_bf16 %0, a[%c1+8:%c1+11], %4, %0\n\t"
      "v_mfma_f32_16x16x32_bf16 %0, a[%c1+12:%c1+15], %5, %0\n\t"
      "s_nop 7"
      : "+v"(acc) : "i"(16 * SL), "v"(bb[0]), "v"(bb[1]), "v"(bb[2]), "v"(bb[3]), "i"(CNT) : "memory", RING_CLOBBERS);
}
template <int SL> __device__ __forceinline__ void ring_fill(u32x4 r, int voff, int soff) {
  asm volatile(
      "buffer_load_dwordx4 a[%c0+0:%c0+3], %1, %2, %3 offen nt\n\t"
      "buffer_load_dwordx4 a[%c0+4:%c0+7], %1, %2, %3 offen offset:256 nt\n\t"
      "buffer_load_dwordx4 a[%c0+8:%c0+11], %1, %2, %3 offen offset:512 nt\n\t"
      "buffer_load_dwordx4 a[%c0+12:%c0+15], %1, %2, %3 offen offset:768 nt"
      :: "i"(16 * SL), "v"(voff), "s"(r), "s"(soff) : "memory", RING_CLOBBERS);
}
#define PACE2 885
#define TEND (LEAD + ((40 - BURST) * PACE + 23 * PACE2 - (7 - P3I) * P3D) / 16)
#define H1T (TEND + H1AT + GAP)
#define WOEV 0
#define WOT (WOAT + ((jb & 1) ? 0 : WOEV))
__device__ __forceinline__ void pace(unsigned& due, unsigned step) {
  asm volatile(
      "1:\n\t"
      "s_memrealtime vcc\n\t"
      "s_waitcnt lgkmcnt(0)\n\t"
      "s_lshl_b32 vcc_lo, vcc_lo, 4\n\t"
      "s_sub_u32 vcc_lo, vcc_lo, %0\n\t"
      "s_cmp_lt_i32 vcc_lo, 0\n\t"
      "s_cbranch_scc0 2f\n\t"
      "s_sleep 1\n\t"
      "s_branch 1b\n"
      "2:\n\t"
      "s_add_u32 %0, %0, %1"
      : "+s"(due) : "s"(step) : "memory", "vcc", "scc");
}
#define DWAITX() do {} while (0)
__device__ __forceinline__ void lds_fill(u32x4 r, int voff, int soff, unsigned dst) {
  unsigned t;
  asm volatile(
      "s_mov_b32 m0, %4\n\ts_add_u32 %0, %4, 768\n\t"
      "buffer_load_dwordx4 %1, %2, %3 offen nt lds\n\t"
      "s_mov_b32 m0, %0\n\ts_add_u32 %0, %0, 768\n\t"
      "buffer_load_dwordx4 %1, %2, %3 offen offset:256 nt lds\n\t"
      "s_mov_b32 m0, %0\n\ts_add_u32 %0, %0, 768\n\t"
      "buffer_load_dwordx4 %1, %2, %3 offen offset:512 nt lds\n\t"
      "s_mov_b32 m0, %0\n\ts_nop 0\n\t"
      "buffer_load_dwordx4 %1, %2, %3 offen offset:768 nt lds"
      : "=&s"(t) : "v"(voff), "s"(r), "s"(soff), "s"(dst) : "memory", "scc", "m0");
}
__device__ __forceinline__ void dma16(const void* g, unsigned dst) {
  asm volatile("s_mov_b32 m0, %1\n\ts_nop 0\n\tglobal_load_lds_dwordx4 %0, off nt" :: "v"(g), "s"(dst) : "memory", "m0");
}
__device__ __forceinline__ void dma16d(const void* g, unsigned dst) {
  asm volatile("s_mov_b32 m0, %1\n\ts_nop 0\n\tglobal_load_lds_dwordx4 %0, off" :: "v"(g), "s"(dst) : "memory", "m0");
}
__device__ __forceinline__ void mfma4(f32x4& d, const u32x4 (&a)[4], const u32x4 (&b)[4]) {
  asm volatile("s_nop 1\n\t"
               "v_mfma_f32_16x16x32_bf16 %0, %1, %5, %0\n\tv_mfma_f32_16x16x32_bf16 %0, %2, %6, %0\n\t"
               "v_mfma_f32_16x16x32_bf16 %0, %3, %7, %0\n\tv_mfma_f32_16x16x32_bf16 %0, %4, %8, %0\n\ts_nop 7"
               : "+v"(d) : "v"(a[0]), "v"(a[1]), "v"(a[2]), "v"(a[3]), "v"(b[0]), "v"(b[1]), "v"(b[2]), "v"(b[3]));
}
typedef short s16x4 __attribute__((ext_vector_type(4)));
__device__ __forceinline__ u32x4 tr_frag(const unsigned char* p, int step) {
  const u32x2 lo = __builtin_bit_cast(u32x2, __builtin_amdgcn_ds_read_tr16_b64_v4i16((LAS s16x4*)p));
  const u32x2 hi = __builtin_bit_cast(u32x2, __builtin_amdgcn_ds_read_tr16_b64_v4i16((LAS s16x4*)(p + step)));
  return u32x4{lo[0], lo[1], hi[0], hi[1]};
}
__device__ __forceinline__ u32x4 make_rsrc(const void* p, unsigned bytes) {
  const u64 a = (u64)p;
  u32x4 r = {(unsigned)__builtin_amdgcn_readfirstlane((unsigned)a), (unsigned)__builtin_amdgcn_readfirstlane((unsigned)(a >> 32) & 0xffffu), bytes, 0x00020000u};
  return r;
}
#define NTS ""
#define LD16(dst, p, OFF) asm volatile("global_load_dwordx4 %0, %1, off offset:%c2" NTS : "=v"(dst) : "v"(p), "i"(OFF) : "memory")
#define LDU16(dst, p, OFF) asm volatile("global_load_ushort %0, %1, off offset:%c2" NTS : "=v"(dst) : "v"(p), "i"(OFF) : "memory")

__device__ __forceinline__ unsigned lds_peek(unsigned addr) {
  unsigned f;
  asm volatile("ds_read_b32 %0, %1\n\ts_waitcnt lgkmcnt(0)" : "=v"(f) : "v"(addr) : "memory");
  return __builtin_amdgcn_readfirstlane(f);
}
__device__ __forceinline__ void lds_arrive(unsigned addr, unsigned one) {
  asm volatile("s_mov_b64 exec, 1\n\tds_add_u32 %0, %1\n\ts_mov_b64 exec, -1" :: "v"(addr), "v"(one) : "memory");
}

__device__ __forceinline__ unsigned lat(const unsigned char* p) {
  unsigned a;
  u64 ta, tb;
  asm volatile("s_waitcnt lgkmcnt(0)\n\ts_memtime %1\n\ts_waitcnt lgkmcnt(0)\n\ts_load_dword %0, %3, 0x0\n\ts_waitcnt lgkmcnt(0)\n\ts_memtime %2\n\ts_waitcnt lgkmcnt(0)"
               : "=&s"(a), "=&s"(ta), "=&s"(tb) : "s"(p) : "memory");
  return (unsigned)tb - (unsigned)ta;
}
__global__ void __launch_bounds__(NTHR, 2)
k_fused(const void*, const void*, const void*, const void*, void*, const void*, const void*, void*, const void*, const void*, const void*) {
  __shared__ __attribute__((aligned(16))) Lds L;
  int tid = threadIdx.x, jb = blockIdx.x;
  const u64 ka = (u64)__builtin_amdgcn_kernarg_segment_ptr();
  u32x16 raw;
  u32x2 ev;
  asm volatile("s_load_dwordx16 %0, %3, 0" : "={s[64:79]}"(raw), "+v"(tid), "+s"(jb) : "s"(ka));
  __builtin_assume(tid >= 0 && tid < NTHR && jb >= 0 && jb < NWG);
#define ARG(T, i) ((T*)(((u64)raw[2 * (i) + 1] << 32) | raw[2 * (i)]))
  const int lane = tid & 63, w = __builtin_amdgcn_readfirstlane(tid >> 6);
  u64 pc0;
  asm volatile("s_getpc_b64 %0" : "=s"(pc0));
  const int kvh = 2 * (jb & 7) + ((jb >> 3) & 1), gi = jb >> 4;
  const int h = 2 * kvh + (gi >> 3), cg = gi & 7;
  const int bo = h >> 3;
  const int c = lane & 15, hl = c >> 3, g4 = lane >> 4;
  const int sw = w - 4;
  static_assert(MAPM[0] == 1 && MAPM[1] == 2 && MAPM[7] == 0x400 && MAPM[8] == 0x800 && MAPM[9] == 0x1040 && MAPM[10] == 0x200 &&
                MAPM[11] == 0x10 && MAPM[12] == 0x20 && MAPM[13] == 0x2080 && MAPM[14] == 0x4100, "am");
  const int am = (w & 3) | ((jb & 7) << 10) | ((jb & 4) << 4) | ((jb & 8) << 6) | (jb & 48) | ((jb >> 6) << 13) | ((jb & 0xc0) << 1);
  const int b = am >> 13, akv = (am >> 9) & 15, split = (__builtin_popcount(jb & SP0) & 1) + 2 * (__builtin_popcount(jb & SP1_) & 1);
  const int tb = (am & 511) << 12;
#define TLO(t, n) ((t) ^ ((BM(n, 0, 2) ^ BM(n, 1, 3) ^ BM(n, 2, 4) ^ BM(n, 3, 5)) << 12))
#define THI(n) (tb ^ ((BM(n, 3, 5) ^ BM(n, 4, 6)) << 12))
#define TOFF(n) TLO(tb, n)
  const size_t kvbase = (size_t)(am >> 9) * SK * DH;
  const int koff = (c >> 2) * 1024 + (4 * g4 + (c & 3)) * 16;
  unsigned t0;
  u32x4 rk, xa[5], xv0, xv1;
  const unsigned short* mw;
  const bool xact = lane < 36;
  const unsigned kv_addr = (unsigned)(size_t)(LAS u32x4*)&L.kv[0][0][0][0] + sw * 16384;
  const unsigned wt_addr = (unsigned)(size_t)(LAS unsigned char*)&L.wt[0][0] + (unsigned)sizeof(L.wt[0]) * w;
  const int r0 = 144 * (gi >> 2), qc = 256 * kvh + 64 * (gi & 3) + 8 * (lane & 7);
  *(u32x4*)&L.sy[4 * min(lane, 2)] = u32x4{0u, 0u, 0u, 0u};
#define NEXTQ "s_add_u32 m0, m0, 1024\n\ts_add_u32 s70, s70, 0x10000\n\ts_addc_u32 s71, s71, 0\n\t"
  asm volatile(
      "s_mov_b32 m0, %[m]\n\t"
      "s_cmp_lt_u32 %[w], 4\n\ts_cbranch_scc1 1f\n\t"
      "s_mov_b32 s90, 0x200000\n\ts_mov_b32 s91, 0x20000\n\t"
      "s_waitcnt lgkmcnt(0)\n\ts_memrealtime s[96:97]\n\t"
      "s_add_u32 s88, s66, %[kb]\n\ts_addc_u32 s89, s67, 0\n\t"
      "buffer_load_dwordx4 %[ko], s[88:91], %[o0] offen nt lds\n\t"
      "s_add_u32 m0, %[m], 768\n\ts_nop 0\n\t"
      "buffer_load_dwordx4 %[ko], s[88:91], %[o0] offen offset:256 nt lds\n\t"
      "s_add_u32 m0, %[m], 1536\n\ts_nop 0\n\t"
      "buffer_load_dwordx4 %[ko], s[88:91], %[o0] offen offset:512 nt lds\n\t"
      "s_add_u32 m0, %[m], 2304\n\ts_nop 0\n\t"
      "buffer_load_dwordx4 %[ko], s[88:91], %[o0] offen offset:768 nt lds\n\t"
      "s_add_u32 m0, %[m], 4096\n\ts_nop 0\n\t"
      "buffer_load_dwordx4 %[ko], s[88:91], %[o1] offen nt lds\n\t"
      "s_add_u32 m0, %[m], 4864\n\ts_nop 0\n\t"
      "buffer_load_dwordx4 %[ko], s[88:91], %[o1] offen offset:256 nt lds\n\t"
      "s_add_u32 m0, %[m], 5632\n\ts_nop 0\n\t"
      "buffer_load_dwordx4 %[ko], s[88:91], %[o1] offen offset:512 nt lds\n\t"
      "s_add_u32 m0, %[m], 6400\n\ts_nop 0\n\t"
      "buffer_load_dwordx4 %[ko], s[88:91], %[o1] offen offset:768 nt lds\n\t"
      "s_branch 2f\n"
      "1:\n\t"
      "s_setprio 3\n\t"
      "global_load_dwordx2 %[mw], %[mo], %[ka] offset:64\n\t"
      "s_waitcnt lgkmcnt(0)\n\ts_memrealtime s[96:97]\n\t"
      "global_load_dwordx4 %[xv0], %[xo], s[64:65]\n\tglobal_load_dwordx4 %[xv1], %[xo], s[64:65] offset:576\n\t"
      "global_load_lds_dwordx4 %[q0], s[70:71] nt\n\t" NEXTQ
      "global_load_lds_dwordx4 %[q0], s[70:71] nt\n\t" NEXTQ
      "global_load_lds_dwordx4 %[q0], s[70:71] nt\n\t" NEXTQ
      "global_load_lds_dwordx4 %[q0], s[70:71] nt\n\t" NEXTQ
      "global_load_lds_dwordx4 %[q4], s[70:71] nt\n\t"
      "global_load_dwordx4 %[a0], %[xp], s[64:65]\n\tglobal_load_dwordx4 %[a1], %[xp], s[64:65] offset:72\n\t"
      "global_load_dwordx4 %[a2], %[xp], s[64:65] offset:144\n\tglobal_load_dwordx4 %[a3], %[xp], s[64:65] offset:216\n\t"
      "global_load_dwordx2 %[a4], %[xq], s[64:65]\n\t"
      "global_load_dwordx2 %[ev], %[z], s[72:73] sc1\n\t"
      "s_barrier\n"
      "2:\n\t"
      "s_waitcnt lgkmcnt(0)\n\ts_mov_b32 %[t0], s96"
      : "+{s[64:79]}"(raw), "=&{s[88:91]}"(rk), [t0] "=s"(t0), [mw] "=&v"(mw), [xv0] "=&v"(xv0), [xv1] "=&v"(xv1), [a0] "=&v"(xa[0]),
        [a1] "=&v"(xa[1]), [a2] "=&v"(xa[2]), [a3] "=&v"(xa[3]), [a4] "=&v"(*(u32x2*)&xa[4]), [ev] "=&v"(ev)
      : [w] "s"(w), [m] "s"(w < 4 ? wt_addr : kv_addr), [kb] "s"((unsigned)kvbase * 2u), [ko] "v"(koff), [o0] "s"(TOFF(0)), [o1] "s"(TOFF(1)),
        [mo] "v"(w < 3 ? 8 * w : 0), [ka] "s"(ka), [xo] "v"((w * DM + 8 * (xact ? lane : 0)) * 2),
        [q0] "v"(((r0 + 36 * w + (lane >> 3)) * 4096 + qc) * 2), [q4] "v"(((r0 + 36 * w + ((lane >> 3) & 3)) * 4096 + qc) * 2),
        [xp] "v"(((c >> 2) * DM + r0 + 8 * g4) * 2), [xq] "v"(((c >> 2) * DM + r0 + 36 * g4 + 32) * 2), [z] "v"(WS_E)
      : "memory", "scc", "m0", "s96", "s97");
  static_assert(SK * DH * 2 == 0x200000, "descriptor");
  const unsigned short *x = ARG(const unsigned short, 0), *vc = ARG(const unsigned short, 2), *wo = ARG(const unsigned short, 5);
  const unsigned short* b2 = ARG(const unsigned short, 6);
  unsigned char* ws = ARG(unsigned char, 4);
  unsigned short* out = ARG(unsigned short, 7);
  const __amdgpu_buffer_rsrc_t rs = __builtin_amdgcn_make_buffer_rsrc((void*)ws, 0, WS_END, 0x00020000);
  const u32x4 rw = make_rsrc(ws, WS_END);
  const unsigned sy_addr = (unsigned)(size_t)(LAS unsigned*)&L.sy[0];
  const unsigned one = 1u;
#define UNPACK16(f, v0, v1) do { _Pragma("unroll") for (int k = 0; k < 4; ++k) { \
    f[2 * k] = bflo(v0[k]); f[2 * k + 1] = bfhi(v0[k]); f[8 + 2 * k] = bflo(v1[k]); f[8 + 2 * k + 1] = bfhi(v1[k]); } } while (0)
#define LN_STATS(f, mean, rstd) do { float s_ = 0.f; _Pragma("unroll") for (int k = 0; k < 16; ++k) s_ += f[k]; \
    mean = wave_sum(xact ? s_ : 0.f) * (1.f / DM); float q_ = 0.f; \
    _Pragma("unroll") for (int k = 0; k < 16; ++k) { f[k] -= mean; q_ = fmaf(f[k], f[k], q_); } \
    rstd = __builtin_amdgcn_rsqf(wave_sum(xact ? q_ : 0.f) * (1.f / DM) + 1e-5f); } while (0)

  if (__builtin_expect(w < 4, 1)) {
    u32x2 e1;
    {
      const int po = c >= 4 || (gi >> 2) >= QABL ? 0x40000000 : WS_Q0 + ((kvh * 4 + (gi >> 2)) * 16 + 4 * (gi & 3) + w) * 256 + g4 * 64 + c * 16;
      asm volatile("s_waitcnt vmcnt(11)" : "+v"(xv0), "+v"(xv1), "+v"(mw) : "v"(po) : "memory");
      {
        float f[16], mean, rstd;
        UNPACK16(f, xv0, xv1);
        LN_STATS(f, mean, rstd);
        if (lane == 0) *(f32x2*)&L.st1[w][0] = f32x2{mean, rstd};
      }
      asm volatile("s_waitcnt vmcnt(0)" : "+v"(xa[0]), "+v"(xa[1]), "+v"(xa[2]), "+v"(xa[3]), "+v"(*(u32x2*)&xa[4]), "+v"(ev) :: "memory");
      lds_arrive(sy_addr + 4, one);
      u32x4 a[4], a4;
#pragma unroll
      for (int s = 0; s < 4; ++s)
#pragma unroll
        for (int k = 0; k < 4; ++k) a[s][k] = (c & 3) == 0 ? xa[s][k] : ((c & 3) == 1 ? 0x3F803F80u : 0u);
#pragma unroll
      for (int k = 0; k < 4; ++k) a4[k] = k >= 2 ? 0u : (c & 3) == 0 ? xa[4][k] : ((c & 3) == 1 ? 0x3F803F80u : 0u);
      const u64 e1w = (((u64)ev[1] << 32) | ev[0]) + 1ull;
      e1 = u32x2{(unsigned)e1w, (unsigned)(e1w >> 32)};
      const int qoff = WS_Q0 + ((akv * 4 + (g4 & (QABL - 1))) * 16 + 4 * w + (c >> 2)) * 256 + b * 64 + (c & 3) * 16;
      const unsigned tw = wide_tag(e1[0]);
      asm volatile("" : "+v"(a[0]), "+v"(a[1]), "+v"(a[2]), "+v"(a[3]), "+v"(a4) : "v"(po), "v"(qoff), "v"(tw));
      while (lds_peek(sy_addr + 4) != 4u) {}
      f32x4 acc = {0.f, 0.f, 0.f, 0.f};
      {
        const unsigned char* tp = &L.wt[0][(8 * g4 + (c >> 2)) * 128 + 32 * w + (c & 3) * 8];
        const unsigned char* tp2 = &L.wt[g4][(32 + (c >> 2)) * 128 + 32 * w + (c & 3) * 8];
        u32x4 bb[4];
#pragma unroll
        for (int s = 0; s < 4; ++s) bb[s] = tr_frag(tp + 5120 * s, 512);
        const u32x2 lo = __builtin_bit_cast(u32x2, __builtin_amdgcn_ds_read_tr16_b64_v4i16((LAS s16x4*)tp2));
        mfma4(acc, a, bb);
        acc = mfma16(a4, u32x4{lo[0], lo[1], 0u, 0u}, acc);
      }
      const f32x2 st = *(const f32x2*)&L.st1[g4][0];
      const float qv = (acc[0] - st[0] * acc[1]) * st[1];
      {
        const unsigned u0 = __float_as_uint(qv) + 0x80u, u1 = dppu<0x104>(u0), u2 = dppu<0x108>(u0), u3 = dppu<0x10C>(u0);
        __builtin_amdgcn_raw_buffer_store_b128(u32x4{perm(u0, u1, 0x07060503u), perm(u1, tw, 0x06050100u), perm(u2, u3, 0x07060503u), perm(u3, tw, 0x06050302u)},
                                               rs, po, 0, QAS);
      }
      if (tid == 192) __builtin_amdgcn_raw_buffer_store_b64(u32x2{t0, tw}, rs, WS_T + 8 * jb, 0, AUX_SC1);
      unsigned dummy;
      asm volatile("buffer_load_dword %0, %1, %2, 0 offen" : "=v"(dummy) : "v"(tid * 128), "s"(make_rsrc((const void*)pc0, PFB)) : "memory");
      u32x4 g;
      __builtin_amdgcn_s_sleep(QS2);
#pragma nounroll
      for (unsigned spins = 0; spins < SPIN_CAP; ++spins) {
        asm volatile("" ::: "memory");
        g = __builtin_amdgcn_raw_buffer_load_b128(rs, qoff, 0, QAL);
        if (__all(perm(g[3], g[1], 0x05040100u) == tw)) break;
        __builtin_amdgcn_s_sleep(QRETRY);
      }
      {
        const float q0 = xrow_sum(__uint_as_float(g[0] & 0xffffff00u)), q1 = xrow_sum(__uint_as_float(perm(g[0], g[1], 0x0403020cu)));
        const float q2 = xrow_sum(__uint_as_float(g[2] & 0xffffff00u)), q3 = xrow_sum(__uint_as_float(perm(g[2], g[3], 0x0403020cu)));
        L.q[64 * w + 4 * (c & 12) + (c & 3) + 4 * g4] = (unsigned short)cvtpk(g4 & 2 ? (g4 & 1 ? q3 : q2) : (g4 & 1 ? q1 : q0), 0.f);
      }
      asm volatile("s_waitcnt vmcnt(0)" :: "v"(dummy) : "memory");
      if (w == 0) *(u32x2*)&L.sy[6] = e1;
      lds_arrive(sy_addr + 8, one);
    }
    __builtin_amdgcn_s_setprio(0);
    while (lds_peek(sy_addr + 8) != 4u) __builtin_amdgcn_s_sleep(4);
    while ((int)((unsigned)__builtin_amdgcn_s_memrealtime() - t0) < WOT) __builtin_amdgcn_s_sleep(2);
    __builtin_amdgcn_s_setprio(3);
    unsigned yw;
    {
      u32x4 xv0, xv1;
      unsigned xs;
      u32x2 tv = {0u, 0u};
      {
        const unsigned short* xr = x + w * DM + 8 * (xact ? lane : 0);
        LD16(xv0, xr, 0); LD16(xv1, xr, 576);
        LDU16(xs, x + w * DM + 72 * cg + gcol(lane < 24 ? lane : 0, bo < 3 ? bo : 0), 0);
        if (w < 3) {
          const unsigned short* mp = mw + 576 * (h & 7) + 8 * lane;
          const unsigned ma = (unsigned)(size_t)(LAS unsigned char*)&L.mw[0][0] + 1152 * w;
          dma16d(mp, ma);
          if (lane < 8) dma16d(mp + 512, ma + 1024);
        }
      }
      {
        const unsigned short* wop = wo + (size_t)(128 * h + 32 * w) * DM + 72 * cg;
#pragma unroll
        for (int t = 0; t < WL0; ++t) {
          const int i = t < 4 ? 64 * t + lane : 256 + (lane & 31);
          const int row = (i * 7282) >> 16;
          dma16(wop + row * DM + 8 * (i - 9 * row), wt_addr + 1024 * t);
        }
      }
      __builtin_amdgcn_s_setprio(0);
      asm volatile("s_waitcnt vmcnt(%c4)" : "+v"(xv0), "+v"(xv1), "+v"(xs), "+v"(tv) : "i"(WL0) : "memory");
      lds_arrive(sy_addr + 32, one);
      f32x2 hv[12];
      {
        float f[16], mean, rstd, mean2, rstd2;
        UNPACK16(f, xv0, xv1);
        LN_STATS(f, mean, rstd);
        u32x4 o0, o1;
#pragma unroll
        for (int k = 0; k < 4; ++k) { o0[k] = cvtpk(f[2 * k] * rstd, f[2 * k + 1] * rstd); o1[k] = cvtpk(f[8 + 2 * k] * rstd, f[8 + 2 * k + 1] * rstd); }
        UNPACK16(f, o0, o1);
        LN_STATS(f, mean2, rstd2);
        const float xn = bflo(cvtpk((bflo(xs) - mean) * rstd, 0.f));
        const unsigned hb = cvtpk(0.f, (xn - mean2) * rstd2);
#pragma unroll
        for (int u = 0; u < 12; ++u)
          hv[u] = f32x2{__builtin_bit_cast(float, __builtin_amdgcn_readlane(hb, 2 * u)), __builtin_bit_cast(float, __builtin_amdgcn_readlane(hb, 2 * u + 1))};
      }
      while (lds_peek(sy_addr + 32) != 4u) __builtin_amdgcn_s_sleep(4);
      const u32x4 m1v = *(const u32x4*)&L.mw[0][16 * lane], mbv = *(const u32x4*)&L.mw[1][16 * lane], m2v = *(const u32x4*)&L.mw[2][16 * lane];
      const unsigned m1s = *(const unsigned short*)&L.mw[0][1024 + 2 * lane], mbs = *(const unsigned short*)&L.mw[1][1024 + 2 * lane],
                     m2s = *(const unsigned short*)&L.mw[2][1024 + 2 * lane];
      constexpr float G0 = -2.0f * 0.7978845608028654f * 1.4426950408889634f, G1 = G0 * 0.044715f;
      f32x2 ya[12];
#pragma unroll
      for (int u = 0; u < 12; ++u) ya[u] = f32x2{0.f, 0.f};
#pragma unroll
      for (int ms = 0; ms < 9; ++ms) {
        const unsigned r1 = ms < 8 ? m1v[ms >> 1] : m1s, rb = ms < 8 ? mbv[ms >> 1] : mbs, r2 = ms < 8 ? m2v[ms >> 1] : m2s;
        const float A1 = (ms & 1) ? bfhi(r1) : bflo(r1), C1 = (ms & 1) ? bfhi(rb) : bflo(rb), A2 = (ms & 1) ? bfhi(r2) : bflo(r2);
#pragma unroll
        for (int u = 0; u < 12; ++u) {
          const f32x2 uu = hv[u] * A1 + C1;
          const f32x2 z = uu * (uu * uu * G1 + G0);
          const f32x2 e = f32x2{__builtin_amdgcn_exp2f(z[0]), __builtin_amdgcn_exp2f(z[1])} + 1.0f;
          const f32x2 gl = uu * f32x2{__builtin_amdgcn_rcpf(e[0]), __builtin_amdgcn_rcpf(e[1])};
          ya[u] = gl * A2 + ya[u];
        }
      }
      float yv = 0.f;
#pragma unroll
      for (int u = 0; u < 24; ++u) { const float s = wave_sum(ya[u >> 1][u & 1]); yv = lane == u ? s : yv; }
      yw = perm(__float_as_uint(yv) + 0x80u, byte_tag(e1[0]), 0x07060503u);
      __builtin_amdgcn_s_setprio(3);
      asm volatile("s_waitcnt vmcnt(0)" : "+v"(yw) :: "memory");
      lds_arrive(sy_addr + 12, one);
    }
    while (lds_peek(sy_addr + 12) != 4u) __builtin_amdgcn_s_sleep(8);
    u32x8 wf[5][2];
#pragma unroll
    for (int nt = 0; nt < 5; ++nt) {
#pragma unroll
      for (int j = 0; j < 2; ++j) {
        const unsigned char* p = &L.wt[0][(16 * (g4 & 1) + 4 * (g4 >> 1) + hl + 2 * (c & 4) + 2 * j) * 144 + (c & 3) * 8 + 32 * nt];
        const u32x4 lo = tr_frag(p, 2 * 5120), hi = tr_frag(p + 5120, 2 * 5120);
        wf[nt][j] = u32x8{lo[0], lo[1], lo[2], lo[3], hi[0], hi[1], hi[2], hi[3]};
      }
      asm volatile("" : "+a"(wf[nt][0]), "+a"(wf[nt][1]));
    }
    int p2o = lane < 24 ? P2OFF(cg, h, w) + lane * 16 : 0x40000000;
    unsigned tb4 = byte_tag(e1[0]);
    unsigned k_hs = 0x07060302u, k_p0 = 0x07060500u, k_p1 = 0x07060501u, k_p2 = 0x07060502u;
    asm volatile("" : "+v"(p2o), "+v"(tb4), "+s"(k_hs), "+s"(k_p0), "+s"(k_p1), "+s"(k_p2));
    {
      int off = P1OFF(h, w, c >> 2) + 64 * g4 + 16 * (c & 3);
      const unsigned tag = __builtin_amdgcn_readfirstlane(wide_tag(e1[0]));
      if (w == 0) {
        if (jb == 0) {
          unsigned m0 = ~0u, m1 = ~0u;
#pragma nounroll
          for (int r = 0; r < NTRY; ++r) {
            const unsigned char* p = ws + WS_Q0 + 2 * 16384 + r * 128;
            m0 = min(m0, lat(p));
            if (m0 < NEARTH) break;
            m1 = min(m1, lat(p + 4096));
            if (m1 < NEARTH || (int)((unsigned)__builtin_amdgcn_s_memrealtime() - t0) > NEND) break;
            __builtin_amdgcn_s_sleep(NGAP);
          }
          float fx = 0.f;
#pragma unroll
          for (int i = 0; i < 2; ++i) {
            const u32x4 tv = __builtin_amdgcn_raw_buffer_load_b128(rs, WS_T + 16 * lane + 1024 * i, 0, AUX_SC1);
#pragma unroll
            for (int k = 0; k < 4; k += 2) { const int d = (int)(tv[k] - t0); fx = fmaxf(fx, tv[k + 1] == tag && d < 200 ? (float)d : 0.f); }
          }
          fx = fmaxf(fx, dpp<XOR1>(fx)); fx = fmaxf(fx, dpp<XOR2>(fx)); fx = fmaxf(fx, dpp<XOR7>(fx)); fx = xrow_max(fmaxf(fx, dpp<ROR8>(fx)));
          if (lane == 0) __builtin_amdgcn_raw_buffer_store_b64(u32x2{((t0 + (int)fx) << 14) | (m1 < m0 ? 8192u : 0u), tag}, rs, WS_N, 0, AUX_SC1);
        }
        while ((int)((unsigned)__builtin_amdgcn_s_memrealtime() - t0) < NAT + (jb >> 3) * 4) __builtin_amdgcn_s_sleep(8);
        u32x2 nv;
#pragma nounroll
        for (unsigned spins = 0; spins < SPIN_CAP; ++spins) {
          asm volatile("" ::: "memory");
          nv = __builtin_amdgcn_raw_buffer_load_b64(rs, lane ? 0x40000000 : WS_N, 0, AUX_SC1);
          if (__builtin_amdgcn_readfirstlane(nv[1]) == tag) break;
          __builtin_amdgcn_s_sleep(16);
        }
        if (lane == 0) L.sy[10] = nv[0] | 1u;
      }
      unsigned near;
      while (!(near = lds_peek(sy_addr + 40))) __builtin_amdgcn_s_sleep(16);
      off ^= near & 8192u; p2o ^= near & 8192u;
      const unsigned t1 = t0 + min(max((int)(near - (t0 << 14)) >> 14, -200), 200);
      asm volatile("" : "+v"(off), "+v"(p2o));
      unsigned n = 0u, ix = (c & 1) ? 0xEEEEu : 0x4444u;
      f32x2 z2 = {0.f, 0.f};
      asm volatile("" : "+s"(n), "+v"(ix), "+v"(z2), "+{v99}"(yw));
      while (lds_peek(sy_addr + 16) < H1GATE) __builtin_amdgcn_s_sleep(SP1);
      __builtin_amdgcn_s_sleep(H1DELAY);
      while ((int)((unsigned)__builtin_amdgcn_s_memrealtime() - t1) < H1T) {}
      asm volatile(
          "buffer_load_dwordx4 v[120:123], %[off], %[rw], 0 offen sc1\n\t"
          "buffer_load_dwordx4 v[124:127], %[off], %[rw], 0 offen offset:256 sc1\n\t"
          "v_pk_mul_f32 v[62:63], %[z], %[z]\n\t"
          "v_pk_mul_f32 v[68:69], %[z], %[z]\n\t"
          "v_pk_mul_f32 v[74:75], %[z], %[z]\n\t"
          "v_pk_mul_f32 v[80:81], %[z], %[z]\n\t"
          "v_pk_mul_f32 v[86:87], %[z], %[z]\n\t"
          "s_waitcnt vmcnt(1)\n\t"
          "v_perm_b32 v102, v123, v121, %[ts]\n\t"
          "s_waitcnt vmcnt(0)\n"
          "1:\n\t"
          "v_mov_b32 v121, v124\n\t"
          "v_mov_b32 v123, v126\n\t"
          "v_perm_b32 v104, v127, v125, %[hs]\n\t"
          "s_nop 0\n\t"
          "v_smfmac_f32_16x16x64_bf16 v[62:65], v[120:123], %[w00], %[ix]\n\t"
          "v_max_f32_dpp v100, v104, v104 row_ror:4 row_mask:0xf bank_mask:0xf\n\t"
          "v_perm_b32 v103, v127, v125, %[ts]\n\t"
          "v_smfmac_f32_16x16x64_bf16 v[68:71], v[120:123], %[w30], %[ix]\n\t"
          "v_max_f32_dpp v100, v100, v100 row_ror:8 row_mask:0xf bank_mask:0xf\n\t"
          "v_sub_f32 v104, v104, v100\n\t"
          "v_smfmac_f32_16x16x64_bf16 v[74:77], v[120:123], %[w10], %[ix]\n\t"
          "v_exp_f32 v104, v104\n\t"
          "v_bitop3_b32 v102, v102, v103, %[tag] bitop3:0x7e\n\t"
          "v_smfmac_f32_16x16x64_bf16 v[60:63], v[120:123], %[w01], %[ix]\n\t"
          "v_add_f32_dpp v100, v104, v104 row_ror:4 row_mask:0xf bank_mask:0xf\n\t"
          "v_cmp_ne_u32 vcc, 0, v102\n\t"
          "v_smfmac_f32_16x16x64_bf16 v[66:69], v[120:123], %[w31], %[ix]\n\t"
          "v_add_f32_dpp v100, v100, v100 row_ror:8 row_mask:0xf bank_mask:0xf\n\t"
          "v_rcp_f32 v100, v100\n\t"
          "v_smfmac_f32_16x16x64_bf16 v[72:75], v[120:123], %[w11], %[ix]\n\t"
          "v_mul_f32 v104, v104, v100\n\t"
          "s_cbranch_vccnz 3f\n\t"
          "v_smfmac_f32_16x16x64_bf16 v[80:83], v[120:123], %[w20], %[ix]\n\t"
          "v_add_f32 v90, v62, v63\n\t"
          "v_mov_b32_dpp v100, v104 row_newbcast:0 row_mask:0x1 bank_mask:0xf\n\t"
          "v_smfmac_f32_16x16x64_bf16 v[86:89], v[120:123], %[w40], %[ix]\n\t"
          "v_mov_b32_dpp v100, v104 row_newbcast:4 row_mask:0x2 bank_mask:0xf\n\t"
          "v_mov_b32_dpp v100, v104 row_newbcast:8 row_mask:0x4 bank_mask:0xf\n\t"
          "v_mov_b32_dpp v100, v104 row_newbcast:12 row_mask:0x8 bank_mask:0xf\n\t"
          "v_smfmac_f32_16x16x64_bf16 v[78:81], v[120:123], %[w21], %[ix]\n\t"
          "v_add_f32 v91, v68, v69\n\t"
          "v_add_f32 v92, v74, v75\n\t"
          "v_smfmac_f32_16x16x64_bf16 v[84:87], v[120:123], %[w41], %[ix]\n\t"
          "v_mul_f32_dpp v93, v91, v100 row_ror:8 row_mask:0xf bank_mask:0xf\n\t"
          "v_pk_mul_f32 v[90:91], v[90:91], v[100:101] op_sel_hi:[1,0]\n\t"
          "v_mul_f32 v92, v92, v100\n\t"
          "s_nop 0\n\t"
          "v_permlane16_swap_b32 v90, v91\n\t"
          "v_permlane16_swap_b32 v92, v93\n\t"
          "v_add_f32 v94, v80, v81\n\t"
          "v_pk_add_f32 v[90:91], v[90:91], v[90:91] op_sel:[0,1] op_sel_hi:[1,0]\n\t"
          "v_add_f32 v95, v86, v87\n\t"
          "v_pk_mul_f32 v[94:95], v[94:95], v[100:101] op_sel_hi:[1,0]\n\t"
          "v_pk_add_f32 v[92:93], v[92:93], v[92:93] op_sel:[0,1] op_sel_hi:[1,0]\n\t"
          "v_permlane32_swap_b32 v90, v91\n\t"
          "v_permlane16_swap_b32 v94, v95\n\t"
          "v_pk_add_f32 v[94:95], v[94:95], v[94:95] op_sel:[0,1] op_sel_hi:[1,0]\n\t"
          "v_permlane32_swap_b32 v92, v93\n\t"
          "v_add_f32 v96, v90, v91\n\t"
          "v_permlane32_swap_b32 v94, v95\n\t"
          "v_add_f32 v97, v92, v93\n\t"
          "v_add_f32 v98, v94, v95\n\t"
          "v_perm_b32 v96, v96, %[tb], %[p0]\n\t"
          "v_perm_b32 v97, v97, %[tb], %[p1]\n\t"
          "v_perm_b32 v98, v98, %[tb], %[p2]\n\t"
          "buffer_store_dwordx4 v[96:99], %[p2o], %[rw], 0 offen sc1\n\t"
          "s_branch 5f\n"
          "3:\n\t"
          "s_cmp_ge_u32 %[n], %c[cap]\n\t"
          "s_cbranch_scc1 5f\n\t"
          "s_mov_b64 exec, vcc\n"
          "2:\n\t"
          "s_sleep %c[gap]\n\t"
          "buffer_load_dwordx4 v[120:123], %[off], %[rw], 0 offen sc1\n\t"
          "buffer_load_dwordx4 v[124:127], %[off], %[rw], 0 offen offset:256 sc1\n\t"
          "s_add_u32 %[n], %[n], 1\n\t"
          "s_cmp_ge_u32 %[n], %c[cap]\n\t"
          "s_waitcnt vmcnt(1)\n\t"
          "v_perm_b32 v102, v123, v121, %[ts]\n\t"
          "s_waitcnt vmcnt(0)\n\t"
          "v_perm_b32 v103, v127, v125, %[ts]\n\t"
          "v_bitop3_b32 v102, v102, v103, %[tag] bitop3:0x7e\n\t"
          "s_cbranch_scc1 4f\n\t"
          "v_cmpx_ne_u32 vcc, 0, v102\n\t"
          "s_cbranch_execnz 2b\n"
          "4:\n\t"
          "s_mov_b64 exec, -1\n\t"
          "v_pk_mul_f32 v[62:63], %[z], %[z]\n\t"
          "v_pk_mul_f32 v[68:69], %[z], %[z]\n\t"
          "v_pk_mul_f32 v[74:75], %[z], %[z]\n\t"
          "v_pk_mul_f32 v[80:81], %[z], %[z]\n\t"
          "v_pk_mul_f32 v[86:87], %[z], %[z]\n\t"
          "v_mov_b32 v102, %[tag]\n\t"
          "s_branch 1b\n"
          "5:"
          : [n] "+s"(n)
          : [off] "v"(off), [rw] "s"(rw), [tag] "s"(tag), [cap] "i"(SPIN_CAP), [gap] "i"(H1GAP), [ts] "s"(0x05040100u), [ix] "v"(ix), [z] "v"(z2), "{v99}"(yw),
            [p2o] "v"(p2o), [tb] "v"(tb4), [hs] "s"(k_hs), [p0] "s"(k_p0), [p1] "s"(k_p1), [p2] "s"(k_p2),
            [w00] "a"(wf[0][0]), [w01] "a"(wf[0][1]), [w10] "a"(wf[1][0]), [w11] "a"(wf[1][1]), [w20] "a"(wf[2][0]), [w21] "a"(wf[2][1]),
            [w30] "a"(wf[3][0]), [w31] "a"(wf[3][1]), [w40] "a"(wf[4][0]), [w41] "a"(wf[4][1])
          : "memory", "scc", "vcc", "v60", "v61", "v62", "v63", "v64", "v65", "v66", "v67", "v68", "v69", "v70", "v71", "v72", "v73", "v74", "v75", "v76", "v77", "v78", "v79", "v80", "v81", "v82", "v83", "v84", "v85", "v86", "v87", "v88", "v89", "v90", "v91", "v92", "v93", "v94", "v95", "v96", "v97", "v98", "v100", "v101", "v102", "v103", "v104", "v120", "v121", "v122", "v123", "v124", "v125", "v126", "v127");
      lds_arrive(sy_addr + 20, one);
    }
  } else {
    __builtin_amdgcn_s_setprio(1);
    asm volatile("s_barrier" ::: "memory");
    unsigned due = (t0 + LEAD + ((jb & 1) ? 0 : EV)) * 16u + sw * STAG + (jb >> 3) * STAGW;
    unsigned pk = (jb & 1) ? PACE : PACE - (EV - EV2) * 16 / (40 - BURST), pv = (jb & 1) ? PACE2 : PACE2 - (EV2 - EV3) * 16 / 24;
#define DBJ 2
#define FILL0(SL) do { if (2 + SL == DBJ) while ((int)((unsigned)__builtin_amdgcn_s_memrealtime() - t0) < DB) __builtin_amdgcn_s_sleep(1); if (2 + SL == GJ) due += 16 * GAP; if (2 + SL >= BURST) pace(due, pk); ring_fill<SL>(rk, koff, TOFF(2 + SL)); } while (0)
    FILL0(0); FILL0(1); FILL0(2); FILL0(3); FILL0(4); FILL0(5); FILL0(6); FILL0(7);
    int pa = (int)(size_t)(LAS float*)&L.pr[0][0][4 * lane], p1v = P1OFF(2 * akv + hl, b, split) + (8 * g4 + (c & 7)) * 16;
    unsigned sel[2][4];
#pragma unroll
    for (int k = 0; k < 4; ++k) {
      sel[0][k] = ((c & 7) >> 1) == k ? ((c & 1) ? 0x01000c0cu : 0x0c0c0100u) : 0x0c0c0c0cu;
      sel[1][k] = ((c & 7) >> 1) == k ? ((c & 1) ? 0x03020c0cu : 0x0c0c0302u) : 0x0c0c0c0cu;
    }
    int paw = pa + 1024 * sw;
    unsigned sya = sy_addr, onev = one, near;
    asm volatile("" : "+v"(pa), "+v"(p1v), "+v"(paw), "+v"(sya), "+v"(onev));
    const u32x4 rv = make_rsrc(vc + kvbase, SK * DH * 2);
    const unsigned sc_addr = (unsigned)(size_t)(LAS f32x4*)&L.sc[sw][0] + (hl * 8 + g4) * 16;
    const u64 wmask = 0x0101010101010101ull;
    pace(due, pk); lds_fill(rk, koff, TOFF(10), kv_addr + 8192);
    pace(due, pk); lds_fill(rk, koff, TOFF(11), kv_addr + 12288);
    while (lds_peek(sy_addr + 8) != 4u) __builtin_amdgcn_s_sleep(1);
    const u32x4 qf = *(const u32x4*)&L.q[hl * 128 + 8 * (4 * g4 + (c & 3))];
    const u32x2 e1 = *(const u32x2*)&L.sy[6];
    const unsigned tw = wide_tag(e1[0]);
    float m = -INFINITY;
#define SCORES(D, SA, OFF) do { f32x4 S_; \
    _Pragma("unroll") for (int t = 0; t < 4; ++t) S_[t] = ((dpp<0x00>(D[t][0]) + dpp<0x55>(D[t][1])) + dpp<0xAA>(D[t][2])) + dpp<0xFF>(D[t][3]); \
    m = fmaxf(fmaxf(m, S_[0]), S_[1]); m = fmaxf(fmaxf(m, S_[2]), S_[3]); \
    asm volatile("s_mov_b64 exec, %2\n\tds_write_b128 %0, %1 offset:%c3\n\ts_mov_b64 exec, -1" :: "v"(SA), "v"(S_), "s"(wmask), "i"(OFF) : "memory"); } while (0)
    {
      u32x4 rc = rk;
      int oc = koff, th = TOFF(8);
#define KSTEP(SL) do { f32x4 D[4]; ring_qk<SL>(D, qf); SCORES(D, sa, (SL >> 1) * 256 + (SL & 1) * 64); \
      if (SL >= 1 && SL <= 3 && n == 24) { if (SL < 3) asm volatile("s_waitcnt vmcnt(%c0)" :: "i"(28 - 4 * SL) : "memory"); } else { \
      DWAITX(); pace(due, pk); ring_fill<SL>(rc, oc, TLO(th, (SL + 4) & 7)); } } while (0)
#define LFILL(SL) do { pace(due, pk); ring_fill<SL>(rv, g4 * 1024 + c * 16, TOFF(SL + 4)); } while (0)
#pragma nounroll
      for (int n = 0;; n += 8) {
        const unsigned sa = sc_addr + (n ? (n + 4) * 128 : 256);
        if (n + 12 == GJ) due += 16 * GAP;
        KSTEP(0); KSTEP(1); KSTEP(2); KSTEP(3);
        {
          const u32x4* kp = &L.kv[sw][n >> 3][0][lane];
          f32x4 D[4];
#pragma unroll
          for (int t = 0; t < 4; ++t) D[t] = f32x4{0.f, 0.f, 0.f, 0.f};
          u32x4 k0 = kp[0], k1 = kp[64], k2 = kp[128], k3 = kp[192];
          asm volatile("s_nop 1\n\t"
                       "v_mfma_f32_16x16x32_bf16 %0, %4, %8, 0\n\tv_mfma_f32_16x16x32_bf16 %1, %5, %8, 0\n\t"
                       "v_mfma_f32_16x16x32_bf16 %2, %6, %8, 0\n\tv_mfma_f32_16x16x32_bf16 %3, %7, %8, 0\n\ts_nop 7"
                       : "=&v"(D[0]), "=&v"(D[1]), "=&v"(D[2]), "=&v"(D[3]) : "v"(k0), "v"(k1), "v"(k2), "v"(k3), "v"(qf));
          const unsigned sl = sc_addr + (n & 16) * 80 + (n & 8) * 8;
          SCORES(D, sl, 0);
        }
        if (n == 24) break;
        if (n + 16 == GJ) due += 16 * GAP;
        if (n == 16) { rc = rv; oc = g4 * 1024 + c * 16; }
        th = THI(n + 16);
        KSTEP(4); KSTEP(5); KSTEP(6); KSTEP(7);
      }
    }
    float l = 0.f;
    {
      LFILL(1);
      m = xrow_max(m);
      const float mc = -m * C2;
      asm volatile("" ::: "memory");
      f32x4 sv[4];
#pragma unroll
      for (int i = 0; i < 4; ++i) sv[i] = L.sc[sw][64 * i + lane];
      unsigned char* pw = &L.pp[sw][hl * 4 + (lane & 3)][(2 * g4 + ((lane >> 2) & 1)) * 8];
#pragma unroll
      for (int i = 0; i < 4; ++i) {
        float p[4];
#pragma unroll
        for (int t = 0; t < 4; ++t) p[t] = __builtin_amdgcn_exp2f(fmaf(sv[i][t], C2, mc));
        l += (p[0] + p[1]) + (p[2] + p[3]);
        *(u32x2*)(pw + 64 * i) = u32x2{cvtpk(p[0], p[1]), cvtpk(p[2], p[3])};
      }
      LFILL(2);
      l += dpp<XOR1>(l); l += dpp<XOR2>(l); l += dpp<XOR7>(l);
      l = xrow_sum(l);
      if ((lane & 55) == 0) { L.pm[hl][sw] = m * C2; L.pl[hl][sw] = l; }
      lds_arrive(sy_addr + 36, one);
    }
    LFILL(3);
    f32x2 fw;
    u32x4 o;
    f32x4 acc = {0.f, 0.f, 0.f, 0.f};
    {
      const int voff = g4 * 1024 + c * 16;
#define VUSE(J, SL, CNT) do { u32x4 bb[4]; asm volatile("" : "+v"(pq[(J) >> 1])); \
      _Pragma("unroll") for (int t = 0; t < 4; ++t) _Pragma("unroll") for (int k = 0; k < 4; ++k) { \
        const unsigned pw_ = pq[(J) >> 1][2 * ((J) & 1) + (t >> 1)]; bb[t][k] = perm(pw_, pw_, sel[t & 1][k]); } \
      ring_pv<(SL) & 7, CNT>(acc, bb); } while (0)
#define VSTEP(I) do { VUSE(I, I + DV + 4, 28 - 4 * DV); pace(due, pv); ring_fill<(I + 4) & 7>(rv, voff, TLO(th, I)); } while (0)
      const unsigned char* pr_ = &L.pp[sw][hl * 4 + g4][0];
      u32x4 pq[4];
#pragma unroll
      for (int i = 0; i < DV / 2; ++i) pq[i] = *(const u32x4*)(pr_ + 16 * i);
      VUSE(0, 4, 28); VUSE(1, 5, 24);
      VUSE(2, 6, 20); VUSE(3, 7, 16);
#pragma nounroll
      for (int n = 0; n < 24; n += 8) {
#pragma unroll
        for (int i = 0; i < 4; ++i) pq[i] = *(const u32x4*)(pr_ + 8 * (n + DV) + 16 * i);
        const int th = THI(n + 8);
        if (P3I == 0 && n == 16) pv -= P3D;
        VSTEP(0); VSTEP(1);
        if (n == PREN) {
          while (lds_peek(sy_addr + 36) != 4u) __builtin_amdgcn_s_sleep(1);
          const f32x4 mm = *(const f32x4*)&L.pm[hl][0], ll = *(const f32x4*)&L.pl[hl][0];
          const float M = fmaxf(fmaxf(mm[0], mm[1]), fmaxf(mm[2], mm[3]));
          float f[4];
#pragma unroll
          for (int i = 0; i < 4; ++i) f[i] = __builtin_amdgcn_exp2f(mm[i] - M);
          const float lsum = fmaf(f[3], ll[3], f[2] * ll[2]) + fmaf(f[1], ll[1], f[0] * ll[0]);
          fw[0] = __builtin_amdgcn_exp2f(m * C2 - M) * __builtin_amdgcn_rcpf(lsum);
          const unsigned lse = __float_as_uint(M + __builtin_amdgcn_logf(lsum));
          o[1] = perm(lse, tw, 0x05040100u);
          o[3] = perm(lse, tw, 0x07060302u);
        }
#define P3(I) do { if (P3I == I && n == 16) pv -= P3D; } while (0)
        P3(2); VSTEP(2); P3(3); VSTEP(3); P3(4); VSTEP(4); P3(5); VSTEP(5); P3(6); VSTEP(6); VSTEP(7);
      }
#pragma unroll
      for (int i = 0; i < 4 - DV / 2; ++i) pq[i] = *(const u32x4*)(pr_ + 8 * (24 + DV) + 16 * i);
      while (!(near = lds_peek(sy_addr + 40))) __builtin_amdgcn_s_sleep(1);
      due = t0 + min(max((int)(near - (t0 << 14)) >> 14, -200), 200) + (TEND + GAP + H2AT);
      near &= 8192u; p1v ^= near;
      VUSE(4 - DV, 0, 12); VUSE(5 - DV, 1, 8);
      VUSE(6 - DV, 2, 4);
      VUSE(7 - DV, 3, 0);
    }
    {
      unsigned ar, sa_;
      asm volatile(
          "v_pk_mul_f32 v[104:105], v[104:105], %3 op_sel_hi:[1,0]\n\t"
          "v_pk_mul_f32 v[106:107], v[106:107], %3 op_sel_hi:[1,0]\n\t"
          "ds_write_b128 %5, v[104:107]\n\t"
          "s_mov_b64 exec, 1\n\tds_add_rtn_u32 %0, %6, %7 offset:16\n\ts_mov_b64 exec, -1\n\t"
          "ds_read_b128 v[108:111], %4\n\tds_read_b128 v[112:115], %4 offset:1024\n\t"
          "ds_read_b128 v[116:119], %4 offset:2048\n\tds_read_b128 v[120:123], %4 offset:3072\n\t"
          "s_waitcnt lgkmcnt(4)\n\t"
          "v_readfirstlane_b32 %1, %0\n\t"
          "s_cmp_lg_u32 %1, 3\n\t"
          "s_cbranch_scc1 1f\n\t"
          "s_waitcnt lgkmcnt(2)\n\t"
          "v_pk_add_f32 v[108:109], v[108:109], v[112:113]\n\tv_pk_add_f32 v[110:111], v[110:111], v[114:115]\n\t"
          "s_waitcnt lgkmcnt(1)\n\t"
          "v_pk_add_f32 v[108:109], v[108:109], v[116:117]\n\tv_pk_add_f32 v[110:111], v[110:111], v[118:119]\n\t"
          "s_waitcnt lgkmcnt(0)\n\t"
          "v_pk_add_f32 v[108:109], v[108:109], v[120:121]\n\tv_pk_add_f32 v[110:111], v[110:111], v[122:123]\n\t"
          "v_cvt_pk_bf16_f32 v124, v108, v109\n\tv_cvt_pk_bf16_f32 v126, v110, v111\n\t"
          "buffer_store_dwordx4 v[124:127], %8, %9, 0 offen sc1\n"
          "1:\n\t"
          "s_waitcnt lgkmcnt(0)\n\ts_nop 0"
          : "=&v"(ar), "=&s"(sa_), "+{v[104:107]}"(acc)
          : "v"(fw), "v"(pa), "v"(paw), "v"(sya), "v"(onev), "v"(p1v), "s"(rw), "{v[124:127]}"(o)
          : "memory", "scc", "v108", "v109", "v110", "v111", "v112", "v113", "v114", "v115", "v116", "v117", "v118", "v119", "v120", "v121", "v122", "v123");
    }
    const int rp = (jb >> 5) & 7, rc = 4 * (jb & 1) + ((jb >> 3) & 3);
#define H2W 1
    if (sw < H2W && rp < 6) {
      const int tp = g4 == 0 ? 0 : g4 == 2 ? 1 : 2, gk = 4 * rp + (lane & 3);
      const int off = sw ? 0x40000000 : (P2OFF(rc, lane >> 2, bo) + 16 * gk) ^ near;
      const bool wr = c < 4 && g4 < 3 && !sw;
      const int oi = bo * DM + 72 * rc + gcol(gk, tp);
      const unsigned tb4 = __builtin_amdgcn_readfirstlane(byte_tag(e1[0]));
      float xres = bf2f(x[oi]), bias = bf2f(b2[0]), nb = bias * -1.4426950408889634f;
      const u32x4 ro = make_rsrc(out, NB * DM * 2);
      int oo = wr ? 2 * oi : 0x40000000, eo = jb == 0 && tid == 256 ? WS_E : 0x40000000;
      u32x2 ev = e1;
      unsigned n = sw ? SPIN_CAP : 0u;
      asm volatile("" : "+v"(xres), "+v"(bias), "+v"(nb), "+v"(oo), "+v"(eo), "+v"(ev), "+s"(n));
      if (!sw) {
        while (lds_peek(sy_addr + 20) < H2GATE) __builtin_amdgcn_s_sleep(SP2);
        while ((int)((unsigned)__builtin_amdgcn_s_memrealtime() - due) < 0) {}
      }
      u64 pd;
      unsigned u0, u1, u2;
      asm volatile(
          "1:\n\t"
          "buffer_load_dwordx4 v[120:123], %6, %7, 0 offen sc1\n\t"
          "buffer_load_dwordx4 v[124:127], %6, %7, %12 offen sc1\n\t"
          "s_add_u32 %4, %4, 1\n\t"
          "s_cmp_ge_u32 %4, %c9\n\t"
          "s_waitcnt vmcnt(1)\n\t"
          "v_perm_b32 %1, v121, v120, %13\n\tv_perm_b32 %2, v123, v122, %14\n\t"
          "v_and_b32 v120, %15, v120\n\tv_and_b32 v121, %15, v121\n\tv_and_b32 v122, %15, v122\n\tv_and_b32 v123, %15, v123\n\t"
          "v_bitop3_b32 %1, %1, %2, %8 bitop3:0x56\n\t"
          "s_waitcnt vmcnt(0)\n\t"
          "v_perm_b32 %2, v125, v124, %13\n\tv_perm_b32 %3, v127, v126, %14\n\t"
          "v_bitop3_b32 %2, %2, %3, %8 bitop3:0x56\n\t"
          "v_or_b32 %1, %1, %2\n\t"
          "s_cbranch_scc1 2f\n\t"
          "v_cmpx_ne_u32 %0, 0, %1\n\t"
          "s_cbranch_execz 2f\n\t"
          "s_sleep %c10\n\t"
          "s_branch 1b\n"
          "2:\n\t"
          "s_mov_b64 exec, -1\n\t"
          "v_and_b32 v127, %15, v127\n\tv_and_b32 v124, %15, v124\n\tv_and_b32 v125, %15, v125\n\t"
          "v_permlane16_swap_b32 v123, v127\n\t"
          "v_and_b32 v126, %15, v126\n\t"
          "v_add_f32 v123, v123, v127\n\t"
          "v_add_f32 v120, v120, v124\n\tv_add_f32 v121, v121, v125\n\t"
          "v_add_f32_dpp v123, v123, v123 row_ror:4 row_mask:0xf bank_mask:0xf\n\t"
          "v_add_f32 v124, v122, v126\n\tv_add_f32 v122, v122, v126\n\t"
          "v_add_f32_dpp v123, v123, v123 row_ror:8 row_mask:0xf bank_mask:0xf\n\t"
          "v_permlane32_swap_b32 v120, v121\n\t"
          "v_fma_f32 %1, %16, v123, %17\n\t"
          "v_permlane32_swap_b32 v122, v124\n\t"
          "v_exp_f32 %1, %1\n\t"
          "v_add_f32 v120, v120, v121\n\tv_add_f32 v122, v122, v124\n\t"
          "v_add_f32 %1, 1.0, %1\n\t"
          "v_rcp_f32 %1, %1\n\t"
          "v_permlane16_swap_b32 v120, v122\n\t"
          "v_add_f32 v120, v120, v122\n\t"
          "v_add_f32 v123, v123, %18\n\t"
          "v_mul_f32 v123, v123, %1\n\t"
          "v_add_f32_dpp v120, v120, v120 row_ror:4 row_mask:0xf bank_mask:0xf\n\t"
          "s_nop 1\n\t"
          "v_add_f32_dpp v120, v120, v120 row_ror:8 row_mask:0xf bank_mask:0xf\n\t"
          "v_fmac_f32 %5, v120, v123\n\t"
          "v_cvt_pk_bf16_f32 %5, %5, %5\n\t"
          "buffer_store_short %5, %19, %20, 0 offen\n\t"
          "buffer_store_dwordx2 %22, %21, %7, 0 offen sc1"
          : "=&s"(pd), "=&v"(u0), "=&v"(u1), "=&v"(u2), "+s"(n), "+v"(xres)
          : "v"(off), "s"(rw), "s"(tb4), "i"(SPIN_CAP), "i"(H2GAP), "i"(0), "s"(2 << 14), "s"(0x0c0c0400u), "s"(0x04000c0cu),
            "s"(0xffffff00u), "s"(-1.4426950408889634f), "v"(nb), "v"(bias), "v"(oo), "s"(ro), "v"(eo), "v"(ev)
          : "memory", "scc", "v120", "v121", "v122", "v123", "v124", "v125", "v126", "v127");
    }
  }
}

extern "C" void kernel_launch(void* const* d_in, const int* in_sizes, int n_in, void* d_out, int out_size, void* d_ws, size_t ws_size,
                              hipStream_t stream) {
  k_fused<<<NWG, NTHR, 0, stream>>>(d_in[0], d_in[1], d_in[2], d_in[3], d_ws, d_in[4], d_in[9], d_out, d_in[6], d_in[7], d_in[8]);
}
```
